```python
import torch

def _softmax(x, axis=-1):
    return torch.softmax(x, dim=axis)
def _gelu(x):
    return torch.nn.functional.gelu(x, approximate="tanh")
def _silu(x):
    return torch.nn.functional.silu(x)
def _sigmoid(x):
    return torch.sigmoid(x)
def _layernorm(x, axis=-1, eps=1e-5):
    m = x.mean(dim=axis, keepdim=True)
    v = x.var(dim=axis, keepdim=True, unbiased=False)
    return (x - m) / torch.sqrt(v + eps)
def _scatter_add(tgt, idx, src, axis=0):
    return tgt.clone().index_add_(axis, idx, src)
def _segment_sum(src, seg, n):
    out = torch.zeros((n,) + src.shape[1:], dtype=src.dtype)
    return out.index_add_(0, seg, src)
def _pad(x, pw):
    flat = tuple(v for pair in reversed(pw) for v in pair)
    return torch.nn.functional.pad(x, flat)

kernel_name = 'concat_heads__spectral_fnet__mha_block__prompt'

def setup_inputs():

    _w = (lambda a: (lambda t: t.to(torch.bfloat16) if t.is_floating_point() else t)(torch.as_tensor(a)))
    def _A_setup():
        B, S, D = 1, 2048, 1024
        ns = torch.arange(S)[:, None] * torch.arange(S)[None, :]
        nd = torch.arange(D)[:, None] * torch.arange(D)[None, :]
        Ws_re = torch.cos(-2.0 * torch.pi * ns / S).to(float)
        Ws_im = torch.sin(-2.0 * torch.pi * ns / S).to(float)
        Wd_re = torch.cos(-2.0 * torch.pi * nd / D).to(float)
        Wd_im = torch.sin(-2.0 * torch.pi * nd / D).to(float)
        return {
            "x": _w((lambda *s: torch.randn(*s, dtype=torch.bfloat16) * 0.05)(B, S, D)),
            "dft_seq_re": _w(Ws_re),
            "dft_seq_im": _w(Ws_im),
            "dft_hid_re": _w(Wd_re),
            "dft_hid_im": _w(Wd_im),
        }
    _A_in = _A_setup()

    def _B_setup():
        B, S, D, H = 1, 2048, 1024, 32
        return {
            "x": _w((lambda *s: torch.randn(*s, dtype=torch.bfloat16) * 0.2)(B, S, D)),
            "wqkv": _w((lambda *s: torch.randn(*s, dtype=torch.bfloat16) * 0.8)(D, 3 * D)),
            "wo": _w((lambda *s: torch.randn(*s, dtype=torch.bfloat16) * 0.2)(D, D)),
        }
    _B_in = _B_setup()

    return {
        "A_x": _A_in["x"],
        "A_dft_seq_re": _A_in["dft_seq_re"],
        "A_dft_seq_im": _A_in["dft_seq_im"],
        "A_dft_hid_re": _A_in["dft_hid_re"],
        "A_dft_hid_im": _A_in["dft_hid_im"],
        "B_x": _B_in["x"],
        "B_wqkv": _B_in["wqkv"],
        "B_wo": _B_in["wo"],
    }

def reference(A_x, A_dft_seq_re, A_dft_seq_im, A_dft_hid_re, A_dft_hid_im, B_x, B_wqkv, B_wo):
    def _A_ref(x, dft_seq_re, dft_seq_im, dft_hid_re, dft_hid_im):
        a = torch.einsum("bsd,ed->bse", x, dft_hid_re)
        b = torch.einsum("bsd,ed->bse", x, dft_hid_im)
        y = torch.einsum("ts,bse->bte", dft_seq_re, a) - torch.einsum("ts,bse->bte", dft_seq_im, b)
        return y

    def _B_ref(x, wqkv, wo):
        B, S, D = x.shape
        H = 32
        Dh = D // H
        qkv = (lambda x, *d: x.permute(*d))((x @ wqkv).reshape(B, S, 3, H, Dh), 2, 0, 3, 1, 4)
        q, k, v = qkv[0], qkv[1], qkv[2]
        scores = torch.einsum("bhqd,bhkd->bhqk", q, k) * (Dh ** -0.5)
        p = _softmax(scores, axis=-1)
        o = torch.einsum("bhqk,bhkd->bhqd", p, v)
        return (lambda x, *d: x.permute(*d))(o, 0, 2, 1, 3).reshape(B, S, D) @ wo

    a = _A_ref(A_x, A_dft_seq_re, A_dft_seq_im, A_dft_hid_re, A_dft_hid_im)
    b = _B_ref(B_x, B_wqkv, B_wo)
    return torch.concatenate([a, b], axis=-1)
```

```cpp
#include <hip/hip_runtime.h>
#include <hip/hip_bf16.h>
typedef unsigned short u16;
__device__ __forceinline__ float b2f(u16 x){ return __uint_as_float(((unsigned)x)<<16); }
__device__ __forceinline__ u16 f2b(float f){ __hip_bfloat16 h = __float2bfloat16(f); return *(u16*)&h; }
__global__ void gemm(const u16* A,int lda,const u16* B,int ldb,int BT,u16* C,int ldc,int M,int N,int K,float sc,int scN,const u16* Cin,int ldcin,float sgn){
  __shared__ float As[32][33], Bs[32][33];
  int tx=threadIdx.x, ty=threadIdx.y; int m=blockIdx.y*32+ty, n=blockIdx.x*32+tx;
  float acc=0;
  for(int k0=0;k0<K;k0+=32){
    As[ty][tx]=b2f(A[(long)(blockIdx.y*32+ty)*lda+k0+tx]);
    if(BT) Bs[tx][ty]=b2f(B[(long)(blockIdx.x*32+ty)*ldb+k0+tx]); else Bs[ty][tx]=b2f(B[(long)(k0+ty)*ldb+blockIdx.x*32+tx]);
    __syncthreads();
    for(int k=0;k<32;++k) acc+=As[ty][k]*Bs[k][tx];
    __syncthreads();
  }
  if(n<scN) acc*=sc;
  acc*=sgn;
  if(Cin) acc+=b2f(Cin[(long)m*ldcin+n]);
  C[(long)m*ldc+n]=f2b(acc);
}
__global__ void attn(const u16* qkv,u16* o){
  int h=blockIdx.y, q=blockIdx.x*64+threadIdx.x;
  __shared__ float Ks[64][32], Vs[64][32];
  float qr[32], acc[32];
  #pragma unroll
  for(int d=0;d<32;++d){ qr[d]=b2f(qkv[(long)q*3072+h*32+d]); acc[d]=0; }
  float m=-1e30f,l=0;
  for(int k0=0;k0<2048;k0+=64){
    __syncthreads();
    for(int d=0;d<32;++d){ Ks[threadIdx.x][d]=b2f(qkv[(long)(k0+threadIdx.x)*3072+1024+h*32+d]); Vs[threadIdx.x][d]=b2f(qkv[(long)(k0+threadIdx.x)*3072+2048+h*32+d]); }
    __syncthreads();
    for(int j=0;j<64;++j){
      float s=0;
      #pragma unroll
      for(int d=0;d<32;++d) s+=qr[d]*Ks[j][d];
      if(s>m+20.f){ float a=exp2f(m-s); l*=a;
        #pragma unroll
        for(int d=0;d<32;++d) acc[d]*=a; m=s; }
      float p=b2f(f2b(exp2f(s-m))); l+=p;
      #pragma unroll
      for(int d=0;d<32;++d) acc[d]+=p*Vs[j][d];
    }
  }
  #pragma unroll
  for(int d=0;d<32;++d) o[(long)q*1024+h*32+d]=f2b(acc[d]/l);
}
extern "C" void kernel_launch(void* const* d_in, const int* in_sizes, int n_in, void* d_out, int out_size, void* d_ws, size_t ws_size, hipStream_t stream) {
  const u16 *xA=(const u16*)d_in[0],*Wsr=(const u16*)d_in[1],*Wsi=(const u16*)d_in[2],*Wdr=(const u16*)d_in[3],*Wdi=(const u16*)d_in[4],*xB=(const u16*)d_in[5],*wqkv=(const u16*)d_in[6],*wo=(const u16*)d_in[7];
  u16* out=(u16*)d_out; u16* ws=(u16*)d_ws;
  u16* a=ws; u16* v=a+2048*1024; u16* y1=v+2048*1024; u16* qkv=y1+2048*1024; u16* o=qkv+2048*3072;
  dim3 b(32,32);
  float c=0.17677669529663687f*1.4426950408889634f;
  gemm<<<dim3(32,64),b,0,stream>>>(xA,1024,Wdr,1024,1,a,1024,2048,1024,1024,1.f,0,nullptr,0,1.f);
  gemm<<<dim3(96,64),b,0,stream>>>(xB,1024,wqkv,3072,0,qkv,3072,2048,3072,1024,c,1024,nullptr,0,1.f);
  gemm<<<dim3(32,64),b,0,stream>>>(Wsr,2048,a,1024,0,y1,1024,2048,1024,2048,1.f,0,nullptr,0,1.f);
  gemm<<<dim3(32,64),b,0,stream>>>(Wsi,2048,xA,1024,0,v,1024,2048,1024,2048,1.f,0,nullptr,0,1.f);
  attn<<<dim3(32,32),64,0,stream>>>(qkv,o);
  gemm<<<dim3(32,64),b,0,stream>>>(v,1024,Wdi,1024,1,out,2048,2048,1024,1024,1.f,0,y1,1024,-1.f);
  gemm<<<dim3(32,64),b,0,stream>>>(o,1024,wo,1024,0,out+1024,2048,2048,1024,1024,1.f,0,nullptr,0,1.f);
}
```

```cpp
#include <hip/hip_runtime.h>
#include <hip/hip_bf16.h>

typedef unsigned short u16;
typedef __attribute__((ext_vector_type(8))) __bf16 bf16x8;
typedef __attribute__((ext_vector_type(2))) __bf16 bf16x2;
typedef __attribute__((ext_vector_type(2))) float f32x2;
typedef __attribute__((ext_vector_type(4))) float f32x4;
typedef __attribute__((ext_vector_type(16))) float f32x16;
typedef __attribute__((ext_vector_type(4))) unsigned u32x4;
typedef __attribute__((ext_vector_type(2))) unsigned u32x2;
typedef __attribute__((ext_vector_type(4))) short s16x4;
#define LAS __attribute__((address_space(3)))
#define GAS __attribute__((address_space(1)))
typedef LAS char* lptr;

#define SBAR() __builtin_amdgcn_sched_barrier(0)
#define PIN(x) asm volatile("" : "+v"(x))
#define MFMA32(a, b, c) __builtin_amdgcn_mfma_f32_32x32x16_bf16((a), (b), (c), 0, 0, 0)
#define MFMA16(a, b, c) __builtin_amdgcn_mfma_f32_16x16x32_bf16((a), (b), (c), 0, 0, 0)
#define BAR_L() asm volatile("s_waitcnt lgkmcnt(0)\n\ts_barrier" ::: "memory")
#define BAR_V(n) asm volatile("s_waitcnt vmcnt(" #n ")\n\ts_barrier" ::: "memory")
#define BAR_VL(n) asm volatile("s_waitcnt vmcnt(" #n ") lgkmcnt(0)\n\ts_barrier" ::: "memory")

#define STAMP(K, W, I)
#define STAMPR(K, W, I)
constexpr int S_ = 2048, D_ = 1024;
enum { OP_ROW = 0, OP_FRAG = 1, OP_TR = 2, OP_TRP = 3 };

__device__ __forceinline__ unsigned pk(float lo, float hi) {
  f32x2 v = {lo, hi};
  return __builtin_bit_cast(unsigned, __builtin_convertvector(v, bf16x2));
}
__device__ __forceinline__ void glds(const char* g, lptr l) {
  __builtin_amdgcn_global_load_lds((const GAS void*)g, (LAS void*)l, 16, 0, 0);
}
template <int KIND>
__device__ __forceinline__ unsigned piece_off(int p, int i, int ld) {
  if constexpr (KIND == OP_ROW) return (8 * p + (i >> 3)) * ld + (((i & 7) ^ (((p & 1) << 2) | (i >> 4))) << 4);
  else if constexpr (KIND == OP_FRAG) return (p >> 2) * ld + (p & 3) * 1024 + i * 16;
  else return (4 * p + (i >> 4)) * ld + (((i & 15) ^ ((i >> 4) << 2)) << 4);
}
template <int KIND>
__device__ __forceinline__ int stage_adv(int ld) {
  if constexpr (KIND == OP_ROW) return 128;
  else if constexpr (KIND == OP_FRAG) return 4096;
  else return 64 * ld;
}
template <int KIND, int NB>
struct FragRd {
  unsigned base[KIND == OP_ROW ? 4 : (KIND == OP_FRAG ? 1 : NB)];
  __device__ __forceinline__ void init(int b0, int lane, unsigned img) {
    const int r = lane & 31, h = lane >> 5;
    if constexpr (KIND == OP_ROW) {
#pragma unroll
      for (int ks = 0; ks < 4; ++ks) base[ks] = img + (b0 * 32 + r) * 128 + (((2 * ks) ^ h ^ ((r >> 1) & 7)) << 4);
    } else if constexpr (KIND == OP_FRAG) {
      base[0] = img + b0 * 4096 + lane * 16;
    } else {
      const int q = (lane & 15) >> 2, p = lane & 3;
#pragma unroll
      for (int b = 0; b < NB; ++b)
        base[b] = img + ((KIND == OP_TR ? 8 : 4) * h + q) * 256 + (((((b0 + b) ^ q) << 2) | (((lane >> 4) & 1) << 1) | (p >> 1)) << 4) + (p & 1) * 8;
    }
  }
  __device__ __forceinline__ bf16x8 rd(lptr st, int b, int ks) const {
    if constexpr (KIND == OP_ROW) return *(const LAS bf16x8*)(st + base[ks] + b * 4096);
    else if constexpr (KIND == OP_FRAG) return *(const LAS bf16x8*)(st + base[0] + b * 4096 + ks * 1024);
    else {
      const s16x4 lo = __builtin_amdgcn_ds_read_tr16_b64_v4i16((LAS s16x4*)(st + base[b] + ks * 4096));
      const s16x4 hi = __builtin_amdgcn_ds_read_tr16_b64_v4i16((LAS s16x4*)(st + base[b] + ks * 4096 + (KIND == OP_TR ? 1024 : 2048)));
      return __builtin_bit_cast(bf16x8, __builtin_shufflevector(lo, hi, 0, 1, 2, 3, 4, 5, 6, 7));
    }
  }
};

__device__ __forceinline__ void pack16(const f32x16& a, float sc, u32x4& c0, u32x4& c1) {
  c0 = u32x4{pk(a[0] * sc, a[1] * sc), pk(a[2] * sc, a[3] * sc), pk(a[4] * sc, a[5] * sc), pk(a[6] * sc, a[7] * sc)};
  c1 = u32x4{pk(a[8] * sc, a[9] * sc), pk(a[10] * sc, a[11] * sc), pk(a[12] * sc, a[13] * sc), pk(a[14] * sc, a[15] * sc)};
}
__device__ __forceinline__ void natural(u32x4& c) {
  auto x = __builtin_amdgcn_permlane32_swap(c[0], c[2], false, false);
  auto y = __builtin_amdgcn_permlane32_swap(c[1], c[3], false, false);
  c = u32x4{x[0], y[0], x[1], y[1]};
}

template <int MI, int NI, bool SWAP, class RA, class RB>
__device__ __forceinline__ void kstep(f32x16 (&acc)[MI][NI], const bf16x8 (&fa)[MI], const bf16x8 (&fb)[NI], bf16x8 (&na)[MI],
                                      bf16x8 (&nb)[NI], const RA& ra, const RB& rb, lptr st, int ks, int first) {
#pragma unroll
  for (int i = 0; i < MI * NI; ++i) {
    const int mi = i / NI, ni = i % NI;
    if (i >= first) acc[mi][ni] = SWAP ? MFMA32(fb[ni], fa[mi], acc[mi][ni]) : MFMA32(fa[mi], fb[ni], acc[mi][ni]);
    if (i == 0) na[0] = ra.rd(st, 0, ks);
    else if (i <= NI) nb[i - 1] = rb.rd(st, i - 1, ks);
    else if (i < MI + NI) na[i - NI] = ra.rd(st, i - NI, ks);
    SBAR();
  }
}

constexpr int K1_STAGE = 49152;
template <int KB>
__device__ __forceinline__ void k1_loader(const char* A, const char* B, int ldb, int lw, int lane, lptr smem) {
  unsigned va[8], vb[4];
#pragma unroll
  for (int j = 0; j < 8; ++j) va[j] = piece_off<OP_ROW>(lw * 8 + j, lane, 2048);
#pragma unroll
  for (int j = 0; j < 4; ++j) vb[j] = piece_off<KB>(lw * 4 + j, lane, ldb);
  const int advb = stage_adv<KB>(ldb);
  auto issue = [&](int t, int buf) {
    const char* a = A + t * 128;
    const char* b = B + (long)t * advb;
    lptr d = smem + buf * K1_STAGE;
#pragma unroll
    for (int j = 0; j < 8; ++j) glds(a + va[j], d + (lw * 8 + j) * 1024);
#pragma unroll
    for (int j = 0; j < 4; ++j) glds(b + vb[j], d + 32768 + (lw * 4 + j) * 1024);
  };
  issue(0, 0); issue(1, 1); issue(2, 2);
  BAR_V(24);
  int buf = 0;
  for (int t = 0; t < 13; ++t) {
    BAR_V(12);
    issue(t + 3, buf);
    buf = buf == 2 ? 0 : buf + 1;
  }
  BAR_V(12);
  BAR_V(0);
}

template <int MI, int NI, int KA, int KB, bool SWAP, int STAGE, int NBUF, int BOFF>
__device__ __forceinline__ void consume(f32x16 (&acc)[MI][NI], int NT, int wm, int wn, int lane, lptr smem) {
  FragRd<KA, MI> ra; ra.init(wm * MI, lane, 0);
  FragRd<KB, NI> rb; rb.init(wn * NI, lane, BOFF);
  bf16x8 a0[MI], b0[NI], a1[MI], b1[NI];
  constexpr int KID = NBUF == 3 ? 0 : 2; const int wv = wm * 2 + wn; (void)wv; (void)KID;
  STAMPR(KID, wv, 46); STAMP(KID, wv, 0);
  BAR_L();
  STAMP(KID, wv, 1);
#pragma unroll
  for (int i = 0; i < MI; ++i) a0[i] = ra.rd(smem, i, 0);
#pragma unroll
  for (int i = 0; i < NI; ++i) b0[i] = rb.rd(smem, i, 0);
  int cur = 0;
  for (int t = 0; t < NT; ++t) {
    lptr st = smem + cur;
    cur = cur == (NBUF - 1) * STAGE ? 0 : cur + STAGE;
    SBAR();
    kstep<MI, NI, SWAP>(acc, a0, b0, a1, b1, ra, rb, st, 1, 0);
    kstep<MI, NI, SWAP>(acc, a1, b1, a0, b0, ra, rb, st, 2, 0);
    kstep<MI, NI, SWAP>(acc, a0, b0, a1, b1, ra, rb, st, 3, 0);
    acc[0][0] = SWAP ? MFMA32(b1[0], a1[0], acc[0][0]) : MFMA32(a1[0], b1[0], acc[0][0]);
    SBAR();
    if (t < NT - 1) BAR_L();
    STAMP(KID, wv, 2 + t);
    SBAR();
    kstep<MI, NI, SWAP>(acc, a1, b1, a0, b0, ra, rb, smem + cur, 0, 1);
  }
  STAMP(KID, wv, 40);
}

__global__ __launch_bounds__(512, 2) void k1(const u16* __restrict__ xB, const u16* __restrict__ wqkv, const u16* __restrict__ xA,
                                             const u16* __restrict__ Wdr, u16* __restrict__ Qf, u16* __restrict__ Kf,
                                             u16* __restrict__ Vf, u16* __restrict__ aT) {
  extern __shared__ __attribute__((aligned(16))) char smem_[];
  lptr smem = (lptr)smem_;
  const int lane = threadIdx.x & 63, w = __builtin_amdgcn_readfirstlane(threadIdx.x >> 6);
  const int xcd = blockIdx.x & 7, i = blockIdx.x >> 3;
  const int kind = xcd >> 1;
  const int tb = (xcd & 1) * 4 + (i & 3);
  const int nb = i >> 2;
  if (w >= 4) {
    if (kind < 3) k1_loader<OP_TR>((const char*)(xB + tb * 256 * D_), (const char*)(wqkv + kind * 1024 + nb * 128), 6144, w - 4, lane, smem);
    else k1_loader<OP_ROW>((const char*)(xA + tb * 256 * D_), (const char*)(Wdr + nb * 128 * D_), 2048, w - 4, lane, smem);
    return;
  }
  const int wm = w >> 1, wn = w & 1;
  f32x16 acc[4][2] = {};
  if (kind < 2) consume<4, 2, OP_ROW, OP_TR, true, K1_STAGE, 3, 32768>(acc, 16, wm, wn, lane, smem);
  else if (kind == 2) consume<4, 2, OP_ROW, OP_TR, false, K1_STAGE, 3, 32768>(acc, 16, wm, wn, lane, smem);
  else consume<4, 2, OP_ROW, OP_ROW, false, K1_STAGE, 3, 32768>(acc, 16, wm, wn, lane, smem);
  u16* dst = kind == 0 ? Qf : kind == 1 ? Kf : kind == 2 ? Vf : aT;
  const float sc = kind == 0 ? 0.17677669529663687f * 1.4426950408889634f : 1.0f;
#pragma unroll
  for (int mi = 0; mi < 4; ++mi)
#pragma unroll
    for (int ni = 0; ni < 2; ++ni) {
      u32x4 c0, c1;
      pack16(acc[mi][ni], sc, c0, c1);
      if (kind == 3) { natural(c0); natural(c1); }
      const int cb = nb * 4 + wn * 2 + ni, tk = tb * 8 + wm * 4 + mi;
      u32x4* p = (u32x4*)(dst + (cb * 64 + tk) * 1024) + lane;
      p[0] = c0; p[64] = c1;
    }
  STAMP(0, w, 41);
  asm volatile("s_waitcnt vmcnt(0)" ::: "memory");
  STAMP(0, w, 42); STAMPR(0, w, 47);
}

constexpr int K2_STAGE = 40960, K2_KOFF = 32768, K2_VOFF = 36864;

template <int KB>
__device__ __forceinline__ void k2_gemm(f32x16 (&acc)[2][2], const char* A, const char* B, int ldb, const char* KV, int gw, int lane, lptr smem) {
  unsigned va[4], vb[4];
#pragma unroll
  for (int j = 0; j < 4; ++j) { va[j] = piece_off<OP_ROW>(gw * 4 + j, lane, 4096); vb[j] = piece_off<KB>(gw * 4 + j, lane, ldb); }
  const int advb = stage_adv<KB>(ldb);
  const unsigned vkv = lane * 16;
  auto issue1 = [&](int t, int off, int j) {
    lptr d = smem + off;
    if (j < 4) glds(A + t * 128 + va[j], d + (gw * 4 + j) * 1024);
    else if (j < 8) glds(B + (long)t * advb + vb[j - 4], d + 16384 + (gw * 4 + j - 4) * 1024);
    else glds(KV + t * 4096 + (j - 8) * 1024 + vkv, d + K2_KOFF + (gw * 2 + j - 8) * 1024);
  };
#pragma unroll
  for (int s = 0; s < 3; ++s)
#pragma unroll
    for (int j = 0; j < 10; ++j) issue1(s, s * K2_STAGE, j);
  const int wm = gw >> 1, wn = gw & 1;
  FragRd<OP_ROW, 2> ra; ra.init(wm * 2, lane, 0);
  FragRd<KB, 2> rb; rb.init(wn * 2, lane, 16384);
  bf16x8 a0[2], b0[2], a1[2], b1[2];
  BAR_V(20);
#pragma unroll
  for (int i = 0; i < 2; ++i) { a0[i] = ra.rd(smem, i, 0); b0[i] = rb.rd(smem, i, 0); }
  int cur = 0;
  for (int t = 0; t < 32; ++t) {
    lptr st = smem + cur;
    const int old = cur;
    cur = cur == 2 * K2_STAGE ? 0 : cur + K2_STAGE;
    SBAR();
    kstep<2, 2, true>(acc, a0, b0, a1, b1, ra, rb, st, 1, 0);
    kstep<2, 2, true>(acc, a1, b1, a0, b0, ra, rb, st, 2, 0);
    kstep<2, 2, true>(acc, a0, b0, a1, b1, ra, rb, st, 3, 0);
    if (t < 30) BAR_VL(10);
    else if (t == 30) BAR_VL(0);
    SBAR();
    kstep<2, 2, true>(acc, a1, b1, a0, b0, ra, rb, smem + cur, 0, 0);
    if (t < 29) {
#pragma unroll
      for (int j = 0; j < 10; ++j) issue1(t + 3, old, j);
    }
  }
}

__device__ __forceinline__ float halfmax(float m) {
  auto rr = __builtin_amdgcn_permlane32_swap(__float_as_uint(m), __float_as_uint(m), false, false);
  return __builtin_fmaxf(__uint_as_float(rr[0]), __uint_as_float(rr[1]));
}
__device__ __forceinline__ float max16(const f32x16& s) {
  float a = __builtin_fmaxf(s[0], s[1]);
#pragma unroll
  for (int r = 2; r < 16; ++r) a = __builtin_fmaxf(a, s[r]);
  return a;
}
__device__ __forceinline__ f32x16 splat16(float v) {
  f32x16 r;
#pragma unroll
  for (int i = 0; i < 16; ++i) r[i] = v;
  return r;
}

constexpr float REF_BIAS = 64.0f;
constexpr float L_LIMIT = 1.099511627776e12f;

__device__ __forceinline__ void attn_safe(const char* Kh, const char* Vh, const char* Qt, int lane, u32x4* o0, u32x4* o1) {
  const bf16x8 q0 = *(const bf16x8*)(Qt + lane * 16), q1 = *(const bf16x8*)(Qt + 1024 + lane * 16);
  float m = -3.0e38f, l = 0.f;
  f32x16 o = {};
  const f32x16 z = {};
#pragma unroll 1
  for (int kt = 0; kt < 64; ++kt) {
    const bf16x8 k0 = *(const bf16x8*)(Kh + kt * 2048 + lane * 16), k1 = *(const bf16x8*)(Kh + kt * 2048 + 1024 + lane * 16);
    const bf16x8 v0 = *(const bf16x8*)(Vh + kt * 2048 + lane * 16), v1 = *(const bf16x8*)(Vh + kt * 2048 + 1024 + lane * 16);
    f32x16 s = MFMA32(k0, q0, z);
    s = MFMA32(k1, q1, s);
    const float mn = __builtin_fmaxf(m, halfmax(max16(s)));
    const float al = __builtin_amdgcn_exp2f(m - mn);
    m = mn;
    float rs = 0.f;
#pragma unroll
    for (int r = 0; r < 16; ++r) { s[r] = __builtin_amdgcn_exp2f(s[r] - mn); o[r] *= al; }
    u32x4 p0, p1;
    pack16(s, 1.0f, p0, p1);
#pragma unroll
    for (int r = 0; r < 4; ++r) {
      rs += __uint_as_float(p0[r] << 16) + __uint_as_float(p0[r] & 0xffff0000u);
      rs += __uint_as_float(p1[r] << 16) + __uint_as_float(p1[r] & 0xffff0000u);
    }
    l = l * al + rs;
    o = MFMA32(v0, __builtin_bit_cast(bf16x8, p0), o);
    o = MFMA32(v1, __builtin_bit_cast(bf16x8, p1), o);
  }
  auto rr = __builtin_amdgcn_permlane32_swap(__float_as_uint(l), __float_as_uint(l), false, false);
  l = __uint_as_float(rr[0]) + __uint_as_float(rr[1]);
  u32x4 c0, c1;
  pack16(o, 1.0f / l, c0, c1);
  *o0 = c0; *o1 = c1;
}

#define EXP2(x) __builtin_amdgcn_exp2f(x)
#define E2(S, i) do { S[i] = EXP2(S[i]); S[i + 1] = EXP2(S[i + 1]); } while (0)
#define UNIT(S, Pn0, Pn1, Sn, KA, KB_, Qa, Qb, NM, O, L, VA, VB, Pp0, Pp1, RD0, RD1, RD2, RD3)                      \
  do {                                                                                                             \
    Sn = MFMA32(KA, Qa, NM); RD0;                                                                                  \
    E2(S, 0); S[2] = EXP2(S[2]); PIN(S[0]); PIN(S[1]); PIN(S[2]); SBAR();                                          \
    Sn = MFMA32(KB_, Qb, Sn); RD1;                                                                                 \
    S[3] = EXP2(S[3]); Pn0[0] = pk(S[0], S[1]); S[4] = EXP2(S[4]); Pn0[1] = pk(S[2], S[3]);                        \
    PIN(Pn0[0]); PIN(Pn0[1]); PIN(S[4]); SBAR();                                                                   \
    O = MFMA32(VA, __builtin_bit_cast(bf16x8, Pp0), O); RD2;                                                       \
    S[5] = EXP2(S[5]); E2(S, 6); Pn0[2] = pk(S[4], S[5]); PIN(Pn0[2]); PIN(S[6]); PIN(S[7]); SBAR();               \
    L = MFMA16(ones, __builtin_bit_cast(bf16x8, Pp0), L); RD3;                                                     \
    E2(S, 8); Pn0[3] = pk(S[6], S[7]); PIN(Pn0[3]); PIN(S[8]); PIN(S[9]); SBAR();                                  \
    O = MFMA32(VB, __builtin_bit_cast(bf16x8, Pp1), O);                                                            \
    E2(S, 10); Pn1[0] = pk(S[8], S[9]); S[12] = EXP2(S[12]); PIN(Pn1[0]); PIN(S[10]); PIN(S[11]); PIN(S[12]); SBAR(); \
    L = MFMA16(ones, __builtin_bit_cast(bf16x8, Pp1), L);                                                          \
    S[13] = EXP2(S[13]); Pn1[1] = pk(S[10], S[11]); E2(S, 14); Pn1[2] = pk(S[12], S[13]); Pn1[3] = pk(S[14], S[15]); \
    PIN(Pn1[1]); PIN(Pn1[2]); PIN(Pn1[3]); SBAR();                                                                 \
  } while (0)
#define RECENTRE(S, NM, O, L)                                                                                      \
  do {                                                                                                             \
    if (__builtin_expect(__any(L[0] > L_LIMIT), 0)) {                                                              \
      int e = __builtin_amdgcn_frexp_expf(L[0]) + (int)REF_BIAS;                                                   \
      e = e > 0 ? e : 0;                                                                                           \
      const float fe = (float)e;                                                                                   \
      _Pragma("unroll") for (int r = 0; r < 16; ++r) { O[r] = __builtin_amdgcn_ldexpf(O[r], -e); S[r] -= fe; NM[r] -= fe; } \
      _Pragma("unroll") for (int r = 0; r < 4; ++r) L[r] = __builtin_amdgcn_ldexpf(L[r], -e);                      \
    }                                                                                                              \
    SBAR();                                                                                                        \
  } while (0)

__device__ __forceinline__ void k2_attn(const char* Qw, const char* Kh, const char* Vh, char* Ow, int lane, lptr smem) {
  __builtin_amdgcn_s_setprio(2);
  const bf16x8 qa0 = *(const bf16x8*)(Qw + lane * 16), qb0 = *(const bf16x8*)(Qw + 1024 + lane * 16);
  const bf16x8 qa1 = *(const bf16x8*)(Qw + 2048 + lane * 16), qb1 = *(const bf16x8*)(Qw + 3072 + lane * 16);
  bf16x8 ones;
  {
    const __bf16 v = (((lane >> 2) ^ (lane >> 4)) & 1) ? (__bf16)0.0f : (__bf16)1.0f;
#pragma unroll
    for (int j = 0; j < 8; ++j) ones[j] = v;
  }
  lptr kb = smem + K2_KOFF + lane * 16;
#define RDK(dst, off, n) dst = *(const LAS bf16x8*)(kb + (off) + (n) * 1024)
#define RDV(dst, off, n) dst = *(const LAS bf16x8*)(kb + (off) + 4096 + (n) * 1024)
  bf16x8 k0a, k0b, k1a, k1b, v0a, v0b, v1a, v1b;
  f32x16 sA, sB, nm0, nm1, o0 = {}, o1 = {};
  f32x4 l0 = {}, l1 = {};
  u32x4 pA0, pA1, pB0 = {}, pB1 = {};
  const int wv = threadIdx.x >> 6; (void)wv;
  STAMPR(1, wv, 46); STAMP(1, wv, 0);
  BAR_L();
  STAMP(1, wv, 1);
  RDK(k0a, 0, 0); RDK(k0b, 0, 1); RDK(k1a, 0, 2); RDK(k1b, 0, 3);
  {
    const f32x16 z = {};
    f32x16 s0 = MFMA32(k0a, qa0, z); s0 = MFMA32(k0b, qb0, s0);
    f32x16 s1 = MFMA32(k1a, qa0, z); s1 = MFMA32(k1b, qb0, s1);
    nm0 = splat16(-(__builtin_ceilf(halfmax(__builtin_fmaxf(max16(s0), max16(s1)))) + REF_BIAS));
    s0 = MFMA32(k0a, qa1, z); s0 = MFMA32(k0b, qb1, s0);
    s1 = MFMA32(k1a, qa1, z); s1 = MFMA32(k1b, qb1, s1);
    nm1 = splat16(-(__builtin_ceilf(halfmax(__builtin_fmaxf(max16(s0), max16(s1)))) + REF_BIAS));
  }
  PIN(nm0); PIN(nm1);
  sA = MFMA32(k0a, qa0, nm0); sA = MFMA32(k0b, qb0, sA);
  v1a = k0a; v1b = k0a;
  int cur = 0;
  for (int t = 0; t < 32; ++t) {
    const int st = cur;
    cur = cur == 2 * K2_STAGE ? 0 : cur + K2_STAGE;
    SBAR();
    RECENTRE(sA, nm0, o0, l0);
    UNIT(sA, pA0, pA1, sB, k0a, k0b, qa1, qb1, nm1, o1, l1, v1a, v1b, pB0, pB1, RDK(k1a, st, 2), RDK(k1b, st, 3), RDV(v0a, st, 0), RDV(v0b, st, 1));
    RECENTRE(sB, nm1, o1, l1);
    UNIT(sB, pB0, pB1, sA, k1a, k1b, qa0, qb0, nm0, o0, l0, v0a, v0b, pA0, pA1, RDV(v1a, st, 2), RDV(v1b, st, 3), (void)0, (void)0);
    STAMP(1, wv + 4, 2 + t);
    if (t < 31) BAR_L();
    STAMP(1, wv, 2 + t);
    SBAR();
    UNIT(sA, pA0, pA1, sB, k1a, k1b, qa1, qb1, nm1, o1, l1, v0a, v0b, pB0, pB1, RDK(k0a, cur, 0), RDK(k0b, cur, 1), (void)0, (void)0);
    UNIT(sB, pB0, pB1, sA, k0a, k0b, qa0, qb0, nm0, o0, l0, v1a, v1b, pA0, pA1, (void)0, (void)0, (void)0, (void)0);
  }
  o1 = MFMA32(v1a, __builtin_bit_cast(bf16x8, pB0), o1);
  o1 = MFMA32(v1b, __builtin_bit_cast(bf16x8, pB1), o1);
  l1 = MFMA16(ones, __builtin_bit_cast(bf16x8, pB0), l1);
  l1 = MFMA16(ones, __builtin_bit_cast(bf16x8, pB1), l1);
  STAMP(1, wv, 40);
  float c0 = l0[0], c1 = l1[0];
#pragma unroll
  for (int r = 0; r < 16; ++r) { c0 += __builtin_fabsf(o0[r]); c1 += __builtin_fabsf(o1[r]); }
  u32x4* d0 = (u32x4*)(Ow) + lane;
  u32x4* d1 = (u32x4*)(Ow + 65536) + lane;
  const unsigned bad = (__any(!(c0 < 3.0e38f) || !(l0[0] > 0.f)) ? 1u : 0u) | (__any(!(c1 < 3.0e38f) || !(l1[0] > 0.f)) ? 2u : 0u);
  if (!(bad & 1)) { u32x4 x, y; pack16(o0, 1.0f / l0[0], x, y); d0[0] = x; d0[64] = y; }
  if (!(bad & 2)) { u32x4 x, y; pack16(o1, 1.0f / l1[0], x, y); d1[0] = x; d1[64] = y; }
  if (__builtin_expect(bad != 0, 0)) {
#pragma unroll 1
    for (int x = 0; x < 2; ++x)
      if ((bad >> x) & 1) attn_safe(Kh, Vh, Qw + x * 2048, lane, d0 + x * 4096, d0 + x * 4096 + 64);
  }
  STAMP(1, wv, 41);
  asm volatile("s_waitcnt vmcnt(0)" ::: "memory");
  STAMP(1, wv, 42); STAMPR(1, wv, 47);
}

__global__ __launch_bounds__(512, 2) void k2(const u16* __restrict__ Qf, const u16* __restrict__ Kf, const u16* __restrict__ Vf,
                                             const u16* __restrict__ Wsr, const u16* __restrict__ Wsi, const u16* __restrict__ aT,
                                             const u16* __restrict__ xA, u16* __restrict__ Of, u16* __restrict__ y1,
                                             u16* __restrict__ vneg) {
  extern __shared__ __attribute__((aligned(16))) char smem_[];
  lptr smem = (lptr)smem_;
  const int lane = threadIdx.x & 63, w = __builtin_amdgcn_readfirstlane(threadIdx.x >> 6);
  const int xcd = blockIdx.x & 7, i = blockIdx.x >> 3;
  const int head = xcd * 4 + (i >> 3), qb = i & 7;
  const char* Kh = (const char*)Kf + head * 131072;
  const char* Vh = (const char*)Vf + head * 131072;
  if (w < 4) {
    const int qt = qb * 8 + w * 2;
    k2_attn((const char*)Qf + (head * 64 + qt) * 2048, Kh, Vh, (char*)Of + qt * 65536 + head * 2048, lane, smem);
    return;
  }
  const int gw = w - 4, type = xcd >> 2, tb = (xcd & 3) * 4 + (i & 3), nb = i >> 2;
  const char* KV = (gw < 2 ? Kh : Vh) + (gw & 1) * 2048;
  f32x16 acc[2][2] = {};
  const int wm = gw >> 1, wn = gw & 1;
  if (type == 0) {
    k2_gemm<OP_FRAG>(acc, (const char*)(Wsr + tb * 128 * S_), (const char*)aT + nb * 4 * 131072, 131072, KV, gw, lane, smem);
    u32x4* p = (u32x4*)((char*)y1 + ((tb * 8 + nb) * 4 + gw) * 8192) + lane;
#pragma unroll
    for (int mi = 0; mi < 2; ++mi)
#pragma unroll
      for (int ni = 0; ni < 2; ++ni) {
        u32x4 c0, c1;
        pack16(acc[mi][ni], 1.0f, c0, c1);
        p[(mi * 2 + ni) * 128] = c0; p[(mi * 2 + ni) * 128 + 64] = c1;
      }
  } else {
    k2_gemm<OP_TR>(acc, (const char*)(Wsi + tb * 128 * S_), (const char*)(xA + nb * 128), 2048, KV, gw, lane, smem);
#pragma unroll
    for (int mi = 0; mi < 2; ++mi)
#pragma unroll
      for (int ni = 0; ni < 2; ++ni) {
        u32x4 c0, c1;
        pack16(acc[mi][ni], -1.0f, c0, c1);
        natural(c0); natural(c1);
        u32x4* p = (u32x4*)((char*)vneg + (tb * 4 + wm * 2 + mi) * 65536 + (nb * 4 + wn * 2 + ni) * 2048) + lane;
        p[0] = c0; p[64] = c1;
      }
  }
}

constexpr int K3_STAGE = 32768;
template <int KB>
__device__ __forceinline__ void k3_loader(const char* A, const char* B, int ldb, int lw, int lane, lptr smem) {
  unsigned va[4], vb[4];
#pragma unroll
  for (int j = 0; j < 4; ++j) { va[j] = piece_off<OP_FRAG>(lw * 4 + j, lane, 65536); vb[j] = piece_off<KB>(lw * 4 + j, lane, ldb); }
  const int advb = stage_adv<KB>(ldb);
  auto issue = [&](int t, int buf) {
    const char* a = A + t * 4096;
    const char* b = B + (long)t * advb;
    lptr d = smem + buf * K3_STAGE;
#pragma unroll
    for (int j = 0; j < 4; ++j) glds(a + va[j], d + (lw * 4 + j) * 1024);
#pragma unroll
    for (int j = 0; j < 4; ++j) glds(b + vb[j], d + 16384 + (lw * 4 + j) * 1024);
  };
  issue(0, 0); issue(1, 1); issue(2, 2); issue(3, 3);
  BAR_V(24);
  for (int t = 0; t < 12; ++t) {
    BAR_V(16);
    issue(t + 4, t & 3);
  }
  BAR_V(16);
  BAR_V(8);
  BAR_V(0);
}

__global__ __launch_bounds__(512, 2) void k3(const u16* __restrict__ vneg, const u16* __restrict__ Wdi, const u16* __restrict__ y1,
                                             const u16* __restrict__ Of, const u16* __restrict__ wo, u16* __restrict__ out) {
  extern __shared__ __attribute__((aligned(16))) char smem_[];
  lptr smem = (lptr)smem_;
  const int lane = threadIdx.x & 63, w = __builtin_amdgcn_readfirstlane(threadIdx.x >> 6);
  const int xcd = blockIdx.x & 7, i = blockIdx.x >> 3;
  const int type = xcd >> 2, tb = (xcd & 3) * 4 + (i & 3), nb = i >> 2;
  if (w >= 4) {
    if (type == 0) k3_loader<OP_ROW>((const char*)vneg + tb * 4 * 65536, (const char*)(Wdi + nb * 128 * D_), 2048, w - 4, lane, smem);
    else k3_loader<OP_TRP>((const char*)Of + tb * 4 * 65536, (const char*)(wo + nb * 128), 2048, w - 4, lane, smem);
    return;
  }
  const int wm = w >> 1, wn = w & 1;
  f32x16 acc[2][2] = {};
  if (type == 0) {
    const u32x4* p = (const u32x4*)((const char*)y1 + ((tb * 8 + nb) * 4 + w) * 8192) + lane;
#pragma unroll
    for (int mi = 0; mi < 2; ++mi)
#pragma unroll
      for (int ni = 0; ni < 2; ++ni)
#pragma unroll
        for (int hf = 0; hf < 2; ++hf) {
          const u32x4 c = p[(mi * 2 + ni) * 128 + hf * 64];
#pragma unroll
          for (int r = 0; r < 4; ++r) {
            acc[mi][ni][hf * 8 + 2 * r] = __uint_as_float(c[r] << 16);
            acc[mi][ni][hf * 8 + 2 * r + 1] = __uint_as_float(c[r] & 0xffff0000u);
          }
        }
    consume<2, 2, OP_FRAG, OP_ROW, true, K3_STAGE, 4, 16384>(acc, 16, wm, wn, lane, smem);
  } else {
    consume<2, 2, OP_FRAG, OP_TRP, true, K3_STAGE, 4, 16384>(acc, 16, wm, wn, lane, smem);
  }
#pragma unroll
  for (int mi = 0; mi < 2; ++mi)
#pragma unroll
    for (int ni = 0; ni < 2; ++ni) {
      u32x4 c0, c1;
      pack16(acc[mi][ni], 1.0f, c0, c1);
      natural(c0); natural(c1);
      const int row = tb * 128 + wm * 64 + mi * 32 + (lane & 31), col = type * 1024 + nb * 128 + wn * 64 + ni * 32 + (lane >> 5) * 8;
      u16* p = out + row * 2048 + col;
      *(u32x4*)p = c0; *(u32x4*)(p + 16) = c1;
    }
  STAMP(2, w, 41);
  asm volatile("s_waitcnt vmcnt(0)" ::: "memory");
  STAMP(2, w, 42); STAMPR(2, w, 47);
}

extern "C" void kernel_launch(void* const* d_in, const int* in_sizes, int n_in, void* d_out, int out_size, void* d_ws,
                              size_t ws_size, hipStream_t stream) {
  const u16 *xA = (const u16*)d_in[0], *Wsr = (const u16*)d_in[1], *Wsi = (const u16*)d_in[2], *Wdr = (const u16*)d_in[3],
            *Wdi = (const u16*)d_in[4], *xB = (const u16*)d_in[5], *wqkv = (const u16*)d_in[6], *wo = (const u16*)d_in[7];
  u16* ws = (u16*)d_ws;
  const size_t T = (size_t)S_ * D_;
  u16 *Qf = ws, *Kf = ws + T, *Vf = ws + 2 * T, *aT = ws + 3 * T, *Of = ws + 4 * T, *y1 = ws + 5 * T, *vneg = ws + 6 * T;
  (void)hipFuncSetAttribute((const void*)k1, hipFuncAttributeMaxDynamicSharedMemorySize, 3 * K1_STAGE);
  (void)hipFuncSetAttribute((const void*)k2, hipFuncAttributeMaxDynamicSharedMemorySize, 3 * K2_STAGE);
  (void)hipFuncSetAttribute((const void*)k3, hipFuncAttributeMaxDynamicSharedMemorySize, 4 * K3_STAGE);
  k1<<<256, 512, 3 * K1_STAGE, stream>>>(xB, wqkv, xA, Wdr, Qf, Kf, Vf, aT);
  k2<<<256, 512, 3 * K2_STAGE, stream>>>(Qf, Kf, Vf, Wsr, Wsi, aT, xA, Of, y1, vneg);
  k3<<<256, 512, 4 * K3_STAGE, stream>>>(vneg, Wdi, y1, Of, wo, (u16*)d_out);
}
```

```cpp
#include <hip/hip_runtime.h>
#include <hip/hip_bf16.h>

typedef unsigned short u16;
typedef __attribute__((ext_vector_type(8))) __bf16 bf16x8;
typedef __attribute__((ext_vector_type(2))) __bf16 bf16x2;
typedef __attribute__((ext_vector_type(2))) float f32x2;
typedef __attribute__((ext_vector_type(4))) float f32x4;
typedef __attribute__((ext_vector_type(16))) float f32x16;
typedef __attribute__((ext_vector_type(4))) unsigned u32x4;
typedef __attribute__((ext_vector_type(2))) unsigned u32x2;
typedef __attribute__((ext_vector_type(4))) short s16x4;
#define LAS __attribute__((address_space(3)))
#define GAS __attribute__((address_space(1)))
typedef LAS char* lptr;

#define SBAR() __builtin_amdgcn_sched_barrier(0)
#define PIN(x) asm volatile("" : "+v"(x))
#define MFMA32(a, b, c) __builtin_amdgcn_mfma_f32_32x32x16_bf16((a), (b), (c), 0, 0, 0)
#define MFMA16(a, b, c) __builtin_amdgcn_mfma_f32_16x16x32_bf16((a), (b), (c), 0, 0, 0)
#define BAR_L() asm volatile("s_waitcnt lgkmcnt(0)\n\ts_barrier" ::: "memory")
#define BAR_V(n) asm volatile("s_waitcnt vmcnt(" #n ")\n\ts_barrier" ::: "memory")
#define BAR_VL(n) asm volatile("s_waitcnt vmcnt(" #n ") lgkmcnt(0)\n\ts_barrier" ::: "memory")

#define STAMP(K, W, I)
#define STAMPR(K, W, I)
#define STAMPI(K, W, I)
constexpr int S_ = 2048, D_ = 1024;
enum { OP_ROW = 0, OP_FRAG = 1, OP_TR = 2, OP_TRP = 3 };

__device__ __forceinline__ unsigned pk(float lo, float hi) {
  f32x2 v = {lo, hi};
  return __builtin_bit_cast(unsigned, __builtin_convertvector(v, bf16x2));
}
__device__ __forceinline__ void glds(const char* g, lptr l) {
  __builtin_amdgcn_global_load_lds((const GAS void*)g, (LAS void*)l, 16, 0, 0);
}
typedef __amdgpu_buffer_rsrc_t rsrc_t;
__device__ __forceinline__ rsrc_t mkrs(const void* p) { return __builtin_amdgcn_make_buffer_rsrc((void*)p, 0, 0x7fffffff, 0x00020000); }
__device__ __forceinline__ void blds(rsrc_t r, unsigned voff, unsigned soff, lptr l) {
  asm volatile("s_mov_b32 m0, %0\n\ts_nop 0\n\tbuffer_load_dwordx4 %1, %2, %3 offen lds"
               :: "s"((unsigned)(unsigned long)l), "v"(voff), "s"(r), "s"(soff) : "memory");
}
template <int KIND>
__device__ __forceinline__ unsigned piece_off(int p, int i, int ld) {
  if constexpr (KIND == OP_ROW) return (8 * p + (i >> 3)) * ld + (((i & 7) ^ (((p & 1) << 2) | (i >> 4))) << 4);
  else if constexpr (KIND == OP_FRAG) return (p >> 2) * ld + (p & 3) * 1024 + i * 16;
  else return (4 * p + (i >> 4)) * ld + (((i & 15) ^ ((i >> 4) << 2)) << 4);
}
template <int KIND>
__device__ __forceinline__ int stage_adv(int ld) {
  if constexpr (KIND == OP_ROW) return 128;
  else if constexpr (KIND == OP_FRAG) return 4096;
  else return 64 * ld;
}
template <int KIND, int NB>
struct FragRd {
  unsigned base[KIND == OP_ROW ? 4 : (KIND == OP_FRAG ? 1 : NB)];
  __device__ __forceinline__ void init(int b0, int lane, unsigned img) {
    const int r = lane & 31, h = lane >> 5;
    if constexpr (KIND == OP_ROW) {
#pragma unroll
      for (int ks = 0; ks < 4; ++ks) base[ks] = img + (b0 * 32 + r) * 128 + (((2 * ks) ^ h ^ ((r >> 1) & 7)) << 4);
    } else if constexpr (KIND == OP_FRAG) {
      base[0] = img + b0 * 4096 + lane * 16;
    } else {
      const int q = (lane & 15) >> 2, p = lane & 3;
#pragma unroll
      for (int b = 0; b < NB; ++b)
        base[b] = img + ((KIND == OP_TR ? 8 : 4) * h + q) * 256 + (((((b0 + b) ^ q) << 2) | (((lane >> 4) & 1) << 1) | (p >> 1)) << 4) + (p & 1) * 8;
    }
  }
  __device__ __forceinline__ bf16x8 rd(lptr st, int b, int ks) const {
    if constexpr (KIND == OP_ROW) return *(const LAS bf16x8*)(st + base[ks] + b * 4096);
    else if constexpr (KIND == OP_FRAG) return *(const LAS bf16x8*)(st + base[0] + b * 4096 + ks * 1024);
    else {
      const s16x4 lo = __builtin_amdgcn_ds_read_tr16_b64_v4i16((LAS s16x4*)(st + base[b] + ks * 4096));
      const s16x4 hi = __builtin_amdgcn_ds_read_tr16_b64_v4i16((LAS s16x4*)(st + base[b] + ks * 4096 + (KIND == OP_TR ? 1024 : 2048)));
      return __builtin_bit_cast(bf16x8, __builtin_shufflevector(lo, hi, 0, 1, 2, 3, 4, 5, 6, 7));
    }
  }
};

__device__ __forceinline__ void pack16(const f32x16& a, float sc, u32x4& c0, u32x4& c1) {
  c0 = u32x4{pk(a[0] * sc, a[1] * sc), pk(a[2] * sc, a[3] * sc), pk(a[4] * sc, a[5] * sc), pk(a[6] * sc, a[7] * sc)};
  c1 = u32x4{pk(a[8] * sc, a[9] * sc), pk(a[10] * sc, a[11] * sc), pk(a[12] * sc, a[13] * sc), pk(a[14] * sc, a[15] * sc)};
}
__device__ __forceinline__ void natural(u32x4& c) {
  auto x = __builtin_amdgcn_permlane32_swap(c[0], c[2], false, false);
  auto y = __builtin_amdgcn_permlane32_swap(c[1], c[3], false, false);
  c = u32x4{x[0], y[0], x[1], y[1]};
}

template <int MI, int NI, bool SWAP, class RA, class RB>
__device__ __forceinline__ void kstep(f32x16 (&acc)[MI][NI], const bf16x8 (&fa)[MI], const bf16x8 (&fb)[NI], bf16x8 (&na)[MI],
                                      bf16x8 (&nb)[NI], const RA& ra, const RB& rb, lptr st, int ks, int first) {
#pragma unroll
  for (int i = 0; i < MI * NI; ++i) {
    const int mi = i / NI, ni = i % NI;
    if (i >= first) acc[mi][ni] = SWAP ? MFMA32(fb[ni], fa[mi], acc[mi][ni]) : MFMA32(fa[mi], fb[ni], acc[mi][ni]);
    if (i == 0) na[0] = ra.rd(st, 0, ks);
    else if (i <= NI) nb[i - 1] = rb.rd(st, i - 1, ks);
    else if (i < MI + NI) na[i - NI] = ra.rd(st, i - NI, ks);
    SBAR();
  }
}

constexpr int K1_STAGE = 49152;
template <int KB>
__device__ __forceinline__ void k1_loader(const char* A, const char* B, int ldb, int lw, int lane, lptr smem) {
  unsigned va[8], vb[4];
#pragma unroll
  for (int j = 0; j < 8; ++j) va[j] = piece_off<OP_ROW>(lw * 8 + j, lane, 2048);
#pragma unroll
  for (int j = 0; j < 4; ++j) vb[j] = piece_off<KB>(lw * 4 + j, lane, ldb);
  const int advb = stage_adv<KB>(ldb);
  const rsrc_t rA = mkrs(A), rB = mkrs(B);
  auto issue = [&](int t, int buf) {
    lptr d = smem + buf * K1_STAGE;
#pragma unroll
    for (int j = 0; j < 8; ++j) blds(rA, va[j], t * 128, d + (lw * 8 + j) * 1024);
#pragma unroll
    for (int j = 0; j < 4; ++j) blds(rB, vb[j], t * advb, d + 32768 + (lw * 4 + j) * 1024);
  };
  issue(0, 0); issue(1, 1); issue(2, 2);
  BAR_V(24);
  int buf = 0;
#pragma unroll 1
  for (int t = 0; t < 13; ++t) {
    BAR_V(12);
    issue(t + 3, buf);
    buf = buf == 2 ? 0 : buf + 1;
  }
  BAR_V(12);
  BAR_V(0);
}

template <int MI, int NI, int KA, int KB, bool SWAP, int STAGE, int NBUF, int BOFF>
__device__ __forceinline__ void consume(f32x16 (&acc)[MI][NI], int NT, int wm, int wn, int lane, lptr smem) {
  FragRd<KA, MI> ra; ra.init(wm * MI, lane, 0);
  FragRd<KB, NI> rb; rb.init(wn * NI, lane, BOFF);
  bf16x8 a0[MI], b0[NI], a1[MI], b1[NI];
  constexpr int KID = NBUF == 3 ? 0 : 2; const int wv = wm * 2 + wn; (void)wv; (void)KID;
  STAMPR(KID, wv, 46); STAMP(KID, wv, 0);
  BAR_L();
  STAMP(KID, wv, 1); STAMPR(KID, wv, 44);
#pragma unroll
  for (int i = 0; i < MI; ++i) a0[i] = ra.rd(smem, i, 0);
#pragma unroll
  for (int i = 0; i < NI; ++i) b0[i] = rb.rd(smem, i, 0);
  int cur = 0;
  for (int t = 0; t < NT; ++t) {
    lptr st = smem + cur;
    cur = cur == (NBUF - 1) * STAGE ? 0 : cur + STAGE;
    SBAR();
    kstep<MI, NI, SWAP>(acc, a0, b0, a1, b1, ra, rb, st, 1, 0);
    kstep<MI, NI, SWAP>(acc, a1, b1, a0, b0, ra, rb, st, 2, 0);
    kstep<MI, NI, SWAP>(acc, a0, b0, a1, b1, ra, rb, st, 3, 0);
    acc[0][0] = SWAP ? MFMA32(b1[0], a1[0], acc[0][0]) : MFMA32(a1[0], b1[0], acc[0][0]);
    SBAR();
    if (t < NT - 1) BAR_L();
    STAMPI(KID, wv, 2 + t);
    SBAR();
    kstep<MI, NI, SWAP>(acc, a1, b1, a0, b0, ra, rb, smem + cur, 0, 1);
  }
  STAMP(KID, wv, 40); STAMPR(KID, wv, 45);
}

__global__ __launch_bounds__(512, 2) void k1(const u16* __restrict__ xB, const u16* __restrict__ wqkv, const u16* __restrict__ xA,
                                             const u16* __restrict__ Wdr, u16* __restrict__ Qf, u16* __restrict__ Kf,
                                             u16* __restrict__ Vf, u16* __restrict__ aT) {
  extern __shared__ __attribute__((aligned(16))) char smem_[];
  lptr smem = (lptr)smem_;
  const int lane = threadIdx.x & 63, w = __builtin_amdgcn_readfirstlane(threadIdx.x >> 6);
  const int xcd = blockIdx.x & 7, i = blockIdx.x >> 3;
  const int kind = xcd >> 1;
  const int tb = (xcd & 1) * 4 + (i & 3);
  const int nb = i >> 2;
  if (w >= 4) {
    if (kind < 3) k1_loader<OP_TR>((const char*)(xB + tb * 256 * D_), (const char*)(wqkv + kind * 1024 + nb * 128), 6144, w - 4, lane, smem);
    else k1_loader<OP_ROW>((const char*)(xA + tb * 256 * D_), (const char*)(Wdr + nb * 128 * D_), 2048, w - 4, lane, smem);
    return;
  }
  const int wm = w >> 1, wn = w & 1;
  f32x16 acc[4][2] = {};
  if (kind < 2) consume<4, 2, OP_ROW, OP_TR, true, K1_STAGE, 3, 32768>(acc, 16, wm, wn, lane, smem);
  else if (kind == 2) consume<4, 2, OP_ROW, OP_TR, false, K1_STAGE, 3, 32768>(acc, 16, wm, wn, lane, smem);
  else consume<4, 2, OP_ROW, OP_ROW, false, K1_STAGE, 3, 32768>(acc, 16, wm, wn, lane, smem);
  u16* dst = kind == 0 ? Qf : kind == 1 ? Kf : kind == 2 ? Vf : aT;
  const float sc = kind == 0 ? 0.17677669529663687f * 1.4426950408889634f : 1.0f;
#pragma unroll
  for (int mi = 0; mi < 4; ++mi)
#pragma unroll
    for (int ni = 0; ni < 2; ++ni) {
      u32x4 c0, c1;
      pack16(acc[mi][ni], sc, c0, c1);
      if (kind == 3) { natural(c0); natural(c1); }
      const int cb = nb * 4 + wn * 2 + ni, tk = tb * 8 + wm * 4 + mi;
      u32x4* p = (u32x4*)(dst + (cb * 64 + tk) * 1024) + lane;
      p[0] = c0; p[64] = c1;
    }
  STAMP(0, w, 41);
  asm volatile("s_waitcnt vmcnt(0)" ::: "memory");
  STAMP(0, w, 42); STAMPR(0, w, 47);
}

#define GP 0
#define AP 2
#define GPRIO __builtin_amdgcn_s_setprio(GP)
#define PACE
constexpr int K2_STAGE = 40960, K2_KOFF = 32768, K2_VOFF = 36864;

template <int KB>
__device__ __forceinline__ void k2_gemm(f32x16 (&acc)[2][2], const char* A, const char* B, int ldb, const char* KV, int gw, int lane, lptr smem) {
  unsigned va[4], vb[4];
#pragma unroll
  for (int j = 0; j < 4; ++j) { va[j] = piece_off<OP_ROW>(gw * 4 + j, lane, 4096); vb[j] = piece_off<KB>(gw * 4 + j, lane, ldb); }
  const int advb = stage_adv<KB>(ldb);
  const unsigned vkv = lane * 16;
  const rsrc_t rA = mkrs(A), rB = mkrs(B), rK = mkrs(KV);
  auto issue1 = [&](int t, int off, int j) {
    lptr d = smem + off;
    if (j < 4) blds(rA, va[j], t * 128, d + (gw * 4 + j) * 1024);
    else if (j < 8) blds(rB, vb[j - 4], t * advb, d + 16384 + (gw * 4 + j - 4) * 1024);
    else blds(rK, vkv, t * 4096 + (j - 8) * 1024, d + K2_KOFF + (gw * 2 + j - 8) * 1024);
  };
  GPRIO;
#pragma unroll
  for (int s = 0; s < 2; ++s)
#pragma unroll
    for (int j = 0; j < 10; ++j) issue1(s, s * K2_STAGE, j);
  const int wm = gw >> 1, wn = gw & 1;
  FragRd<OP_ROW, 2> ra; ra.init(wm * 2, lane, 0);
  FragRd<KB, 2> rb; rb.init(wn * 2, lane, 16384);
  bf16x8 a0[2], b0[2], a1[2], b1[2];
  BAR_V(10);
#pragma unroll
  for (int i = 0; i < 2; ++i) { a0[i] = ra.rd(smem, i, 0); b0[i] = rb.rd(smem, i, 0); }
  int cur = 0, old = 2 * K2_STAGE;
#define GSTEP(FA, FB, NA, NB, ST, KS, J0)                                                                    \
  do {                                                                                                       \
    acc[0][0] = MFMA32(FB[0], FA[0], acc[0][0]); SBAR(); NA[0] = ra.rd(ST, 0, KS); NB[0] = rb.rd(ST, 0, KS); \
    if (ld && J0 >= 0) issue1(t + 2, old, J0); PACE; SBAR();                                                 \
    acc[0][1] = MFMA32(FB[1], FA[0], acc[0][1]); SBAR(); NB[1] = rb.rd(ST, 1, KS); NA[1] = ra.rd(ST, 1, KS); \
    if (ld && J0 >= 0) issue1(t + 2, old, J0 + 1); PACE; SBAR();                                             \
    acc[1][0] = MFMA32(FB[0], FA[1], acc[1][0]); SBAR();                                                     \
    if (ld && J0 >= 0 && J0 < 8) issue1(t + 2, old, J0 + 2); PACE; SBAR();                                   \
    acc[1][1] = MFMA32(FB[1], FA[1], acc[1][1]); SBAR();                                                     \
    if (ld && J0 >= 0 && J0 < 8) issue1(t + 2, old, J0 + 3); PACE; SBAR();                                   \
  } while (0)
#define GITER(LD, BAR)                                  \
  do {                                                  \
    lptr st = smem + cur;                               \
    constexpr bool ld = LD;                             \
    SBAR();                                             \
    GSTEP(a0, b0, a1, b1, st, 1, 0);                    \
    GSTEP(a1, b1, a0, b0, st, 2, 4);                    \
    GSTEP(a0, b0, a1, b1, st, 3, 8);                    \
    old = cur;                                          \
    cur = cur == 2 * K2_STAGE ? 0 : cur + K2_STAGE;     \
    BAR;                                                \
    SBAR();                                             \
    GSTEP(a1, b1, a0, b0, smem + cur, 0, -1);           \
  } while (0)
#pragma unroll 1
  for (int t = 0; t < 30; ++t) GITER(true, BAR_VL(10));
#pragma unroll 1
  for (int t = 30; t < 32; ++t) GITER(false, if (t == 30) BAR_VL(0));
}

__device__ __forceinline__ float halfmax(float m) {
  auto rr = __builtin_amdgcn_permlane32_swap(__float_as_uint(m), __float_as_uint(m), false, false);
  return __builtin_fmaxf(__uint_as_float(rr[0]), __uint_as_float(rr[1]));
}
__device__ __forceinline__ float max16(const f32x16& s) {
  float a = __builtin_fmaxf(s[0], s[1]);
#pragma unroll
  for (int r = 2; r < 16; ++r) a = __builtin_fmaxf(a, s[r]);
  return a;
}
__device__ __forceinline__ f32x16 splat16(float v) {
  f32x16 r;
#pragma unroll
  for (int i = 0; i < 16; ++i) r[i] = v;
  return r;
}

#define RBIAS 64.0f
constexpr float REF_BIAS = RBIAS;
constexpr float L_LIMIT = 1.099511627776e12f;

__device__ __forceinline__ void attn_safe(const char* Kh, const char* Vh, const char* Qt, int lane, u32x4* o0, u32x4* o1) {
  const bf16x8 q0 = *(const bf16x8*)(Qt + lane * 16), q1 = *(const bf16x8*)(Qt + 1024 + lane * 16);
  float m = -3.0e38f, l = 0.f;
  f32x16 o = {};
  const f32x16 z = {};
#pragma unroll 1
  for (int kt = 0; kt < 64; ++kt) {
    const bf16x8 k0 = *(const bf16x8*)(Kh + kt * 2048 + lane * 16), k1 = *(const bf16x8*)(Kh + kt * 2048 + 1024 + lane * 16);
    const bf16x8 v0 = *(const bf16x8*)(Vh + kt * 2048 + lane * 16), v1 = *(const bf16x8*)(Vh + kt * 2048 + 1024 + lane * 16);
    f32x16 s = MFMA32(k0, q0, z);
    s = MFMA32(k1, q1, s);
    const float mn = __builtin_fmaxf(m, halfmax(max16(s)));
    const float al = __builtin_amdgcn_exp2f(m - mn);
    m = mn;
    float rs = 0.f;
#pragma unroll
    for (int r = 0; r < 16; ++r) { s[r] = __builtin_amdgcn_exp2f(s[r] - mn); o[r] *= al; }
    u32x4 p0, p1;
    pack16(s, 1.0f, p0, p1);
#pragma unroll
    for (int r = 0; r < 4; ++r) {
      rs += __uint_as_float(p0[r] << 16) + __uint_as_float(p0[r] & 0xffff0000u);
      rs += __uint_as_float(p1[r] << 16) + __uint_as_float(p1[r] & 0xffff0000u);
    }
    l = l * al + rs;
    o = MFMA32(v0, __builtin_bit_cast(bf16x8, p0), o);
    o = MFMA32(v1, __builtin_bit_cast(bf16x8, p1), o);
  }
  auto rr = __builtin_amdgcn_permlane32_swap(__float_as_uint(l), __float_as_uint(l), false, false);
  l = __uint_as_float(rr[0]) + __uint_as_float(rr[1]);
  u32x4 c0, c1;
  pack16(o, 1.0f / l, c0, c1);
  *o0 = c0; *o1 = c1;
}

#define EXP2(x) __builtin_amdgcn_exp2f(x)
#define E2(S, i) do { S[i] = EXP2(S[i]); S[i + 1] = EXP2(S[i + 1]); } while (0)
#define X1(S, i) do { S[i] = EXP2(S[i]); PIN(S[i]); SBAR(); } while (0)
#define CV(P, j, S, i) do { P[j] = pk(S[i], S[i + 1]); PIN(P[j]); SBAR(); } while (0)
#define BC(x) __builtin_bit_cast(bf16x8, x)
#define SUMP(L, P) asm volatile("v_mfma_f32_16x16x32_bf16 %0, %1, %2, %0" : "+v"(L) : "v"(ones), "v"(P))
#define XT(T, S, i) do { T = EXP2(S[i]); PIN(T); SBAR(); } while (0)
#define CT(P, j, A, B) do { P[j] = pk(A, B); PIN(P[j]); SBAR(); } while (0)
#define UNIT(S, Pn0, Pn1, Sn, KA, KB_, Qa, Qb, NM, O, L, VA, VB, Pp0, Pp1, RD0, RD1, RD2, RD3)   \
  do {                                                                                          \
    Sn = MFMA32(KA, Qa, NM); SBAR(); RD0; SBAR();                                               \
    X1(S, 0); CT(Pp1, 2, tl0, tl1); X1(S, 1); X1(S, 2);                                         \
    Sn = MFMA32(KB_, Qb, Sn); SBAR(); RD1; SBAR();                                              \
    X1(S, 3); CT(Pp1, 3, tl2, tl3); X1(S, 4); X1(S, 5);                                         \
    O = MFMA32(VA, BC(Pp0), O); SBAR(); RD2; SBAR();                                            \
    CV(Pn0, 0, S, 0); X1(S, 6); X1(S, 7); CV(Pn0, 1, S, 2);                                     \
    SUMP(L, Pp0); SBAR(); RD3; SBAR();                                                          \
    X1(S, 8); X1(S, 9); CV(Pn0, 2, S, 4);                                                       \
    O = MFMA32(VB, BC(Pp1), O); SBAR();                                                         \
    X1(S, 10); CV(Pn0, 3, S, 6); X1(S, 11); XT(tl0, S, 12);                                     \
    SUMP(L, Pp1); SBAR();                                                                       \
    CV(Pn1, 0, S, 8); XT(tl1, S, 13); XT(tl2, S, 14); CV(Pn1, 1, S, 10); XT(tl3, S, 15);        \
  } while (0)
#define RECENTRE(NM, O, L, FE)                                                                  \
  do {                                                                                          \
    int e = __builtin_amdgcn_frexp_expf(L[0]) + (int)REF_BIAS;                                  \
    e = e > 0 ? e : 0;                                                                          \
    FE = (float)e;                                                                              \
    _Pragma("unroll") for (int r = 0; r < 16; ++r) { O[r] = __builtin_amdgcn_ldexpf(O[r], -e); NM[r] -= FE; } \
    _Pragma("unroll") for (int r = 0; r < 4; ++r) L[r] = __builtin_amdgcn_ldexpf(L[r], -e);     \
  } while (0)

__device__ __forceinline__ void k2_attn(const char* Qw, const char* Kh, const char* Vh, char* Ow, int lane, lptr smem) {
  __builtin_amdgcn_s_setprio(AP);
  const bf16x8 qa0 = *(const bf16x8*)(Qw + lane * 16), qb0 = *(const bf16x8*)(Qw + 1024 + lane * 16);
  const bf16x8 qa1 = *(const bf16x8*)(Qw + 2048 + lane * 16), qb1 = *(const bf16x8*)(Qw + 3072 + lane * 16);
  bf16x8 ones;
  {
    const __bf16 v = (((lane >> 2) ^ (lane >> 4)) & 1) ? (__bf16)0.0f : (__bf16)1.0f;
#pragma unroll
    for (int j = 0; j < 8; ++j) ones[j] = v;
  }
  lptr kb = smem + K2_KOFF + lane * 16;
#define RDK(dst, off, n) dst = *(const LAS bf16x8*)(kb + (off) + (n) * 1024)
#define RDV(dst, off, n) dst = *(const LAS bf16x8*)(kb + (off) + 4096 + (n) * 1024)
  bf16x8 k0a, k0b, k1a, k1b, v0a, v0b, v1a, v1b;
  f32x16 sA, sB, nm0, nm1, o0 = {}, o1 = {};
  f32x4 l0 = {}, l1 = {};
  u32x4 pA0, pA1, pB0 = {}, pB1 = {};
  const int wv = threadIdx.x >> 6; (void)wv;
  STAMPR(1, wv, 46); STAMP(1, wv, 0);
  BAR_L();
  STAMP(1, wv, 1); STAMPR(1, wv, 44);
  RDK(k0a, 0, 0); RDK(k0b, 0, 1); RDK(k1a, 0, 2); RDK(k1b, 0, 3);
  {
    const f32x16 z = {};
    f32x16 s0 = MFMA32(k0a, qa0, z); s0 = MFMA32(k0b, qb0, s0);
    f32x16 s1 = MFMA32(k1a, qa0, z); s1 = MFMA32(k1b, qb0, s1);
    nm0 = splat16(-(__builtin_ceilf(halfmax(__builtin_fmaxf(max16(s0), max16(s1)))) + REF_BIAS));
    s0 = MFMA32(k0a, qa1, z); s0 = MFMA32(k0b, qb1, s0);
    s1 = MFMA32(k1a, qa1, z); s1 = MFMA32(k1b, qb1, s1);
    nm1 = splat16(-(__builtin_ceilf(halfmax(__builtin_fmaxf(max16(s0), max16(s1)))) + REF_BIAS));
  }
  PIN(nm0); PIN(nm1);
  sA = MFMA32(k0a, qa0, nm0); sA = MFMA32(k0b, qb0, sA);
  v1a = k0a; v1b = k0a;
  int cur = 0, t = 0;
  float tl0 = 0.f, tl1 = 0.f, tl2 = 0.f, tl3 = 0.f;
#pragma unroll 1
  while (t < 32) {
    bool trig;
#pragma unroll 1
    do {
      const int st = cur;
      cur = cur == 2 * K2_STAGE ? 0 : cur + K2_STAGE;
      SBAR();
      UNIT(sA, pA0, pA1, sB, k0a, k0b, qa1, qb1, nm1, o1, l1, v1a, v1b, pB0, pB1, RDK(k1a, st, 2), RDK(k1b, st, 3), RDV(v0a, st, 0), RDV(v0b, st, 1));
      UNIT(sB, pB0, pB1, sA, k1a, k1b, qa0, qb0, nm0, o0, l0, v0a, v0b, pA0, pA1, RDV(v1a, st, 2), RDV(v1b, st, 3), (void)0, (void)0);
      STAMPI(1, wv + 4, 2 + t);
      if (t < 31) BAR_L();
      STAMPI(1, wv, 2 + t);
      SBAR();
      UNIT(sA, pA0, pA1, sB, k1a, k1b, qa1, qb1, nm1, o1, l1, v0a, v0b, pB0, pB1, RDK(k0a, cur, 0), RDK(k0b, cur, 1), (void)0, (void)0);
      UNIT(sB, pB0, pB1, sA, k0a, k0b, qa0, qb0, nm0, o0, l0, v1a, v1b, pA0, pA1, (void)0,
           trig = __any(l0[0] + l1[0] > L_LIMIT), (void)0, (void)0);
      ++t;
    } while (t < 32 && !trig);
    if (t >= 32) break;
    asm volatile("s_nop 7" : "+v"(l0), "+v"(l1));
    pB1[2] = pk(tl0, tl1); pB1[3] = pk(tl2, tl3);
    o1 = MFMA32(v1a, BC(pB0), o1); o1 = MFMA32(v1b, BC(pB1), o1);
    l1 = MFMA16(ones, BC(pB0), l1); l1 = MFMA16(ones, BC(pB1), l1);
    pB0 = u32x4{}; pB1 = u32x4{}; tl0 = 0.f; tl1 = 0.f; tl2 = 0.f; tl3 = 0.f;
    float f0, f1;
    RECENTRE(nm0, o0, l0, f0);
    RECENTRE(nm1, o1, l1, f1);
#pragma unroll
    for (int r = 0; r < 16; ++r) sA[r] -= f0;
    asm volatile("s_nop 1" : "+v"(l0), "+v"(l1), "+v"(pB0), "+v"(pB1));
  }
  asm volatile("s_nop 7" : "+v"(l0), "+v"(l1));
  pB1[2] = pk(tl0, tl1); pB1[3] = pk(tl2, tl3);
  o1 = MFMA32(v1a, __builtin_bit_cast(bf16x8, pB0), o1);
  o1 = MFMA32(v1b, __builtin_bit_cast(bf16x8, pB1), o1);
  l1 = MFMA16(ones, __builtin_bit_cast(bf16x8, pB0), l1);
  l1 = MFMA16(ones, __builtin_bit_cast(bf16x8, pB1), l1);
  STAMP(1, wv, 40); STAMPR(1, wv, 45);
  float c0 = l0[0], c1 = l1[0];
#pragma unroll
  for (int r = 0; r < 16; ++r) { c0 += __builtin_fabsf(o0[r]); c1 += __builtin_fabsf(o1[r]); }
  u32x4* d0 = (u32x4*)(Ow) + lane;
  u32x4* d1 = (u32x4*)(Ow + 65536) + lane;
  const unsigned bad = (__any(!(c0 < 3.0e38f) || !(l0[0] > 0.f)) ? 1u : 0u) | (__any(!(c1 < 3.0e38f) || !(l1[0] > 0.f)) ? 2u : 0u);
  if (!(bad & 1)) { u32x4 x, y; pack16(o0, 1.0f / l0[0], x, y); d0[0] = x; d0[64] = y; }
  if (!(bad & 2)) { u32x4 x, y; pack16(o1, 1.0f / l1[0], x, y); d1[0] = x; d1[64] = y; }
  if (__builtin_expect(bad != 0, 0)) {
#pragma unroll 1
    for (int x = 0; x < 2; ++x)
      if ((bad >> x) & 1) attn_safe(Kh, Vh, Qw + x * 2048, lane, d0 + x * 4096, d0 + x * 4096 + 64);
  }
  STAMP(1, wv, 41);
  asm volatile("s_waitcnt vmcnt(0)" ::: "memory");
  STAMP(1, wv, 42); STAMPR(1, wv, 47);
}

__global__ __launch_bounds__(512, 2) void k2(const u16* __restrict__ Qf, const u16* __restrict__ Kf, const u16* __restrict__ Vf,
                                             const u16* __restrict__ Wsr, const u16* __restrict__ Wsi, const u16* __restrict__ aT,
                                             const u16* __restrict__ xA, u16* __restrict__ Of, u16* __restrict__ y1,
                                             u16* __restrict__ vneg) {
  extern __shared__ __attribute__((aligned(16))) char smem_[];
  lptr smem = (lptr)smem_;
  const int lane = threadIdx.x & 63, w = __builtin_amdgcn_readfirstlane(threadIdx.x >> 6);
  const int xcd = blockIdx.x & 7, i = blockIdx.x >> 3;
  const int head = xcd * 4 + (i >> 3), qb = i & 7;
  const char* Kh = (const char*)Kf + head * 131072;
  const char* Vh = (const char*)Vf + head * 131072;
  if (w < 4) {
    const int qt = qb * 8 + w * 2;
    k2_attn((const char*)Qf + (head * 64 + qt) * 2048, Kh, Vh, (char*)Of + qt * 65536 + head * 2048, lane, smem);
    return;
  }
  const int gw = w - 4, type = xcd >> 2, tb = (xcd & 3) * 4 + (i & 3), nb = i >> 2;
  const char* KV = (gw < 2 ? Kh : Vh) + (gw & 1) * 2048;
  f32x16 acc[2][2] = {};
  const int wm = gw >> 1, wn = gw & 1;
  if (type == 0) {
    k2_gemm<OP_FRAG>(acc, (const char*)(Wsr + tb * 128 * S_), (const char*)aT + nb * 4 * 131072, 131072, KV, gw, lane, smem);
    u32x4* p = (u32x4*)((char*)y1 + ((tb * 8 + nb) * 4 + gw) * 8192) + lane;
#pragma unroll
    for (int mi = 0; mi < 2; ++mi)
#pragma unroll
      for (int ni = 0; ni < 2; ++ni) {
        u32x4 c0, c1;
        pack16(acc[mi][ni], 1.0f, c0, c1);
        p[(mi * 2 + ni) * 128] = c0; p[(mi * 2 + ni) * 128 + 64] = c1;
      }
  } else {
    k2_gemm<OP_TR>(acc, (const char*)(Wsi + tb * 128 * S_), (const char*)(xA + nb * 128), 2048, KV, gw, lane, smem);
#pragma unroll
    for (int mi = 0; mi < 2; ++mi)
#pragma unroll
      for (int ni = 0; ni < 2; ++ni) {
        u32x4 c0, c1;
        pack16(acc[mi][ni], -1.0f, c0, c1);
        natural(c0); natural(c1);
        u32x4* p = (u32x4*)((char*)vneg + (tb * 4 + wm * 2 + mi) * 65536 + (nb * 4 + wn * 2 + ni) * 2048) + lane;
        p[0] = c0; p[64] = c1;
      }
  }
}

constexpr int K3_STAGE = 32768;
template <int KB>
__device__ __forceinline__ void k3_loader(const char* A, const char* B, int ldb, int lw, int lane, lptr smem) {
  unsigned va[4], vb[4];
#pragma unroll
  for (int j = 0; j < 4; ++j) { va[j] = piece_off<OP_FRAG>(lw * 4 + j, lane, 65536); vb[j] = piece_off<KB>(lw * 4 + j, lane, ldb); }
  const int advb = stage_adv<KB>(ldb);
  const rsrc_t rA = mkrs(A), rB = mkrs(B);
  auto issue = [&](int t, int buf) {
    lptr d = smem + buf * K3_STAGE;
#pragma unroll
    for (int j = 0; j < 4; ++j) blds(rA, va[j], t * 4096, d + (lw * 4 + j) * 1024);
#pragma unroll
    for (int j = 0; j < 4; ++j) blds(rB, vb[j], t * advb, d + 16384 + (lw * 4 + j) * 1024);
  };
  issue(0, 0); issue(1, 1); issue(2, 2); issue(3, 3);
  BAR_V(24);
#pragma unroll 1
  for (int t = 0; t < 12; ++t) {
    BAR_V(16);
    issue(t + 4, t & 3);
  }
  BAR_V(16);
  BAR_V(8);
  BAR_V(0);
}

__global__ __launch_bounds__(512, 2) void k3(const u16* __restrict__ vneg, const u16* __restrict__ Wdi, const u16* __restrict__ y1,
                                             const u16* __restrict__ Of, const u16* __restrict__ wo, u16* __restrict__ out) {
  extern __shared__ __attribute__((aligned(16))) char smem_[];
  lptr smem = (lptr)smem_;
  const int lane = threadIdx.x & 63, w = __builtin_amdgcn_readfirstlane(threadIdx.x >> 6);
  const int xcd = blockIdx.x & 7, i = blockIdx.x >> 3;
  const int type = xcd >> 2, tb = (xcd & 3) * 4 + (i & 3), nb = i >> 2;
  if (w >= 4) {
    if (type == 0) k3_loader<OP_ROW>((const char*)vneg + tb * 4 * 65536, (const char*)(Wdi + nb * 128 * D_), 2048, w - 4, lane, smem);
    else k3_loader<OP_TRP>((const char*)Of + tb * 4 * 65536, (const char*)(wo + nb * 128), 2048, w - 4, lane, smem);
    return;
  }
  const int wm = w >> 1, wn = w & 1;
  f32x16 acc[2][2] = {};
  if (type == 0) {
    const u32x4* p = (const u32x4*)((const char*)y1 + ((tb * 8 + nb) * 4 + w) * 8192) + lane;
#pragma unroll
    for (int mi = 0; mi < 2; ++mi)
#pragma unroll
      for (int ni = 0; ni < 2; ++ni)
#pragma unroll
        for (int hf = 0; hf < 2; ++hf) {
          const u32x4 c = p[(mi * 2 + ni) * 128 + hf * 64];
#pragma unroll
          for (int r = 0; r < 4; ++r) {
            acc[mi][ni][hf * 8 + 2 * r] = __uint_as_float(c[r] << 16);
            acc[mi][ni][hf * 8 + 2 * r + 1] = __uint_as_float(c[r] & 0xffff0000u);
          }
        }
    consume<2, 2, OP_FRAG, OP_ROW, true, K3_STAGE, 4, 16384>(acc, 16, wm, wn, lane, smem);
  } else {
    consume<2, 2, OP_FRAG, OP_TRP, true, K3_STAGE, 4, 16384>(acc, 16, wm, wn, lane, smem);
  }
#pragma unroll
  for (int mi = 0; mi < 2; ++mi)
#pragma unroll
    for (int ni = 0; ni < 2; ++ni) {
      u32x4 c0, c1;
      pack16(acc[mi][ni], 1.0f, c0, c1);
      natural(c0); natural(c1);
      const int row = tb * 128 + wm * 64 + mi * 32 + (lane & 31), col = type * 1024 + nb * 128 + wn * 64 + ni * 32 + (lane >> 5) * 8;
      u16* p = out + row * 2048 + col;
      *(u32x4*)p = c0; *(u32x4*)(p + 16) = c1;
    }
  STAMP(2, w, 41);
  asm volatile("s_waitcnt vmcnt(0)" ::: "memory");
  STAMP(2, w, 42); STAMPR(2, w, 47);
}

extern "C" void kernel_launch(void* const* d_in, const int* in_sizes, int n_in, void* d_out, int out_size, void* d_ws,
                              size_t ws_size, hipStream_t stream) {
  const u16 *xA = (const u16*)d_in[0], *Wsr = (const u16*)d_in[1], *Wsi = (const u16*)d_in[2], *Wdr = (const u16*)d_in[3],
            *Wdi = (const u16*)d_in[4], *xB = (const u16*)d_in[5], *wqkv = (const u16*)d_in[6], *wo = (const u16*)d_in[7];
  u16* ws = (u16*)d_ws;
  const size_t T = (size_t)S_ * D_;
  u16 *Qf = ws, *Kf = ws + T, *Vf = ws + 2 * T, *aT = ws + 3 * T, *Of = ws + 4 * T, *y1 = ws + 5 * T, *vneg = ws + 6 * T;
  (void)hipFuncSetAttribute((const void*)k1, hipFuncAttributeMaxDynamicSharedMemorySize, 3 * K1_STAGE);
  (void)hipFuncSetAttribute((const void*)k2, hipFuncAttributeMaxDynamicSharedMemorySize, 3 * K2_STAGE);
  (void)hipFuncSetAttribute((const void*)k3, hipFuncAttributeMaxDynamicSharedMemorySize, 4 * K3_STAGE);
  k1<<<256, 512, 3 * K1_STAGE, stream>>>(xB, wqkv, xA, Wdr, Qf, Kf, Vf, aT);
  k2<<<256, 512, 3 * K2_STAGE, stream>>>(Qf, Kf, Vf, Wsr, Wsi, aT, xA, Of, y1, vneg);
  k3<<<256, 512, 4 * K3_STAGE, stream>>>(vneg, Wdi, y1, Of, wo, (u16*)d_out);
}
```

```cpp
#include <hip/hip_runtime.h>
#include <hip/hip_bf16.h>
#include <type_traits>

typedef unsigned short u16;
typedef __attribute__((ext_vector_type(8))) __bf16 bf16x8;
typedef __attribute__((ext_vector_type(2))) __bf16 bf16x2;
typedef __attribute__((ext_vector_type(2))) float f32x2;
typedef __attribute__((ext_vector_type(4))) float f32x4;
typedef __attribute__((ext_vector_type(16))) float f32x16;
typedef __attribute__((ext_vector_type(4))) unsigned u32x4;
typedef __attribute__((ext_vector_type(2))) unsigned u32x2;
typedef __attribute__((ext_vector_type(4))) short s16x4;
#define LAS __attribute__((address_space(3)))
#define GAS __attribute__((address_space(1)))
typedef LAS char* lptr;
typedef GAS unsigned gu32;
typedef __amdgpu_buffer_rsrc_t rsrc_t;

#define SBAR() __builtin_amdgcn_sched_barrier(0)
#define PIN(x) asm volatile("" : "+v"(x))
#define MFMA32(a, b, c) __builtin_amdgcn_mfma_f32_32x32x16_bf16((a), (b), (c), 0, 0, 0)
#define MFMA16(a, b, c) __builtin_amdgcn_mfma_f32_16x16x32_bf16((a), (b), (c), 0, 0, 0)
#define BC(x) __builtin_bit_cast(bf16x8, x)
#define RLX_AGENT __ATOMIC_RELAXED, __HIP_MEMORY_SCOPE_AGENT
template <int N> __device__ __forceinline__ void bar_vl() { asm volatile("s_waitcnt vmcnt(%0) lgkmcnt(0)\n\ts_barrier" ::"n"(N) : "memory"); }

#define STAMP(I)
#define ST2(W, I)

constexpr int S_ = 2048, D_ = 1024, SLOT = 40960, LDS_BYTES = 4 * SLOT;
enum { OP_ROW = 0, OP_FRAG = 1, OP_TR = 2, OP_TRP = 3 };

__device__ __forceinline__ unsigned pk(float lo, float hi) {
  f32x2 v = {lo, hi};
  return __builtin_bit_cast(unsigned, __builtin_convertvector(v, bf16x2));
}
__device__ __forceinline__ rsrc_t mkrs(const void* p, bool on = true) {
  return __builtin_amdgcn_make_buffer_rsrc((void*)p, 0, on ? 0x7fffffff : 0, 0x00020000);
}
template <bool DEV = false>
__device__ __forceinline__ void blds(rsrc_t r, unsigned voff, unsigned soff, unsigned l) {
  if constexpr (DEV) asm volatile("s_mov_b32 m0, %0\n\ts_nop 0\n\tbuffer_load_dwordx4 %1, %2, %3 offen sc1 lds" ::"s"(l), "v"(voff), "s"(r), "s"(soff) : "memory");
  else asm volatile("s_mov_b32 m0, %0\n\ts_nop 0\n\tbuffer_load_dwordx4 %1, %2, %3 offen lds" ::"s"(l), "v"(voff), "s"(r), "s"(soff) : "memory");
}
__device__ __forceinline__ bf16x8 ld_dev(rsrc_t r, unsigned voff, unsigned soff = 0) {
  return __builtin_bit_cast(bf16x8, __builtin_amdgcn_raw_buffer_load_b128(r, voff, soff, 16));
}
__device__ __forceinline__ void st_wt(rsrc_t r, unsigned voff, u32x4 v) { __builtin_amdgcn_raw_buffer_store_b128(v, r, voff, 0, 16); }

template <int KIND>
__device__ __forceinline__ unsigned piece_off(int p, int i, int ld) {
  if constexpr (KIND == OP_ROW) return (8 * p + (i >> 3)) * ld + (((i & 7) ^ (((p & 1) << 2) | (i >> 4))) << 4);
  else if constexpr (KIND == OP_FRAG) return (p >> 1) * ld + (p & 1) * 1024 + i * 16;
  else if constexpr (KIND == OP_TR) return (8 * p + (i >> 3)) * ld + (((i & 7) ^ ((((i >> 4) & 1) | ((p & 1) << 1)) << 1)) << 4);
  else return (8 * p + (i >> 3)) * ld + (((i & 7) ^ ((i >> 4) << 1)) << 4);
}
template <int KIND, int NBASE>
struct FragRd {
  static constexpr int NB = KIND == OP_ROW ? 2 : (KIND == OP_FRAG ? 1 : NBASE);
  static constexpr int OPS = KIND >= OP_TR ? 2 : 1;
  unsigned base[NB];
  __device__ __forceinline__ void init(int b0, int lane, unsigned img) {
    const int r = lane & 15, g = lane >> 4;
    if constexpr (KIND == OP_ROW) {
#pragma unroll
      for (int ks = 0; ks < 2; ++ks) base[ks] = img + (b0 * 16 + r) * 128 + (((4 * ks + g) ^ ((r >> 1) & 7)) << 4);
    } else if constexpr (KIND == OP_FRAG) {
      base[0] = img + b0 * 2048 + lane * 16;
    } else {
      const int q = r >> 2, p = lane & 3, f = ((q >> 1) & 1) | ((g & 1) << 1);
      const int row = (KIND == OP_TR ? 8 * g : 16 * (g >> 1) + 4 * (g & 1)) + q;
#pragma unroll
      for (int b = 0; b < NBASE; ++b) base[b] = img + row * 128 + (((((b0 + b) ^ f) << 1) | (p >> 1)) << 4) + (p & 1) * 8;
    }
  }
  __device__ __forceinline__ void step(int k, int d) { base[k] += d; PIN(base[k]); }
  __device__ __forceinline__ void rd(u32x4& f, int b, int ks, int half) const {
    if constexpr (KIND == OP_ROW) f = *(const LAS u32x4*)(unsigned long)(base[ks] + b * 2048);
    else if constexpr (KIND == OP_FRAG) f = *(const LAS u32x4*)(unsigned long)(base[0] + b * 2048 + ks * 1024);
    else {
      const u32x2 x = __builtin_bit_cast(u32x2, __builtin_amdgcn_ds_read_tr16_b64_v4i16(
          (LAS s16x4*)(unsigned long)(base[b % NBASE] + (b / NBASE) * 8192 + ks * 4096 + half * (KIND == OP_TR ? 512 : 1024))));
      f[2 * half] = x[0]; f[2 * half + 1] = x[1];
    }
  }
};

__device__ __forceinline__ u32x4 pack2(const f32x4& x, const f32x4& y, float sc) {
  return u32x4{pk(x[0] * sc, x[1] * sc), pk(x[2] * sc, x[3] * sc), pk(y[0] * sc, y[1] * sc), pk(y[2] * sc, y[3] * sc)};
}
__device__ __forceinline__ u32x4 natural(u32x4 c) {
  auto x = __builtin_amdgcn_permlane32_swap(c[0], c[2], false, false);
  auto y = __builtin_amdgcn_permlane32_swap(c[1], c[3], false, false);
  auto p = __builtin_amdgcn_permlane16_swap(x[0], x[1], false, false);
  auto q = __builtin_amdgcn_permlane16_swap(y[0], y[1], false, false);
  return u32x4{p[0], q[0], p[1], q[1]};
}

__device__ __forceinline__ void bar_l() { asm volatile("s_waitcnt lgkmcnt(0)\n\ts_barrier" ::: "memory"); }
template <int N> __device__ __forceinline__ void bar_v() { asm volatile("s_waitcnt vmcnt(%0)\n\ts_barrier" ::"n"(N) : "memory"); }
__device__ __forceinline__ void drain() { asm volatile("s_waitcnt vmcnt(0)" ::: "memory"); }
__device__ __forceinline__ void set_flag(gu32* flag, unsigned tag) {
  if ((threadIdx.x & 255) == 0) __hip_atomic_store(flag, tag, RLX_AGENT);
}
constexpr int MAIL = 32768 + 4096;
__device__ __forceinline__ void mail(gu32* flags, int n, int lane, unsigned lds0) {
  asm volatile("s_mov_b32 m0, %0\n\ts_nop 0\n\tbuffer_load_dword %1, %2, 0 offen sc1 lds"
               ::"s"(lds0 + MAIL), "v"((lane < n ? lane : 0) * 4), "s"(mkrs((const void*)flags)) : "memory");
}
__device__ __forceinline__ void await(gu32* flags, int n, unsigned tag, int lane, lptr smem) {
  if (__builtin_expect(!__all(*(volatile LAS unsigned*)(smem + MAIL + lane * 4) == tag), 0)) {
#pragma unroll 1
    for (unsigned spins = 0; spins < 400000u; ++spins) {
      if (__all(__hip_atomic_load(flags + (lane < n ? lane : 0), RLX_AGENT) == tag)) break;
      __builtin_amdgcn_s_sleep(2);
    }
  }
  __builtin_amdgcn_fence(__ATOMIC_ACQUIRE, "wavefront");
}
#define WAIT_LGKM(n) __builtin_amdgcn_s_waitcnt(0xC07F | ((n) << 8))

template <int MI, int NI, int NSWAP, int S0, int NT, int ND, class RA, class RB>
__device__ __forceinline__ void consume(f32x4 (&acc)[MI][NI], RA& ra, RB& rb) {
  u32x4 a0[MI], b0[NI], a1[MI], b1[NI];
  constexpr int NM = MI * NI, NBO = NI * RB::OPS, NR = MI + NBO, NU = RA::NB + RB::NB;
  auto rdop = [&](u32x4(&na)[MI], u32x4(&nb)[NI], int ks, int z) {
    if (z == 0) ra.rd(na[0], 0, ks, 0);
    else if (z <= NBO) rb.rd(nb[(z - 1) / RB::OPS], (z - 1) / RB::OPS, ks, (z - 1) % RB::OPS);
    else ra.rd(na[z - NBO], z - NBO, ks, 0);
  };
  auto mma = [&](const u32x4(&fa)[MI], const u32x4(&fb)[NI], int i) {
    const int mi = i / NI, ni = i % NI;
    acc[mi][ni] = ni < NSWAP ? MFMA16(BC(fb[ni]), BC(fa[mi]), acc[mi][ni]) : MFMA16(BC(fa[mi]), BC(fb[ni]), acc[mi][ni]);
    SBAR();
  };
#pragma unroll
  for (int z = 0; z < NR; ++z) { rdop(a0, b0, 0, z); SBAR(); }
  int slot = S0 & 3;
#pragma unroll 1
  for (int seg = ND ? 0 : 1; seg < 2; ++seg) {
#pragma unroll 1
    for (int t = seg ? NT - ND : ND; t > 0; --t) {
      slot = (slot + 1) & 3;
      const int d = slot == 0 ? -3 * SLOT : SLOT;
      WAIT_LGKM(MI - 1);
      SBAR();
#pragma unroll
      for (int i = 0; i < NM + 2; ++i) {
        if (i < NM) mma(a0, b0, i);
        else { if (i == NM) { WAIT_LGKM(0); SBAR(); } mma(a1, b1, i - NM); }
        if (i < NR) rdop(a1, b1, 1, i);
        else {
          constexpr int G = NM + 2 - NR;
#pragma unroll
          for (int u = (i - NR) * NU / G; u < (i - NR + 1) * NU / G; ++u) { if (u < RA::NB) ra.step(u, d); else rb.step(u - RA::NB, d); }
        }
        SBAR();
      }
      asm volatile("s_barrier" ::: "memory");
      SBAR();
      constexpr int G1 = NM - 2;
#pragma unroll
      for (int i = 0; i < G1; ++i) {
        if (NR > G1 && i < NR - G1) { rdop(a0, b0, 0, 2 * i); SBAR(); }
        mma(a1, b1, i + 2);
        const int z = NR > G1 ? (i < NR - G1 ? 2 * i + 1 : i + NR - G1) : i;
        if (z < NR) rdop(a0, b0, 0, z);
        SBAR();
      }
    }
    if (ND) drain();
  }
}

__device__ __forceinline__ void pack16(const f32x16& a, float sc, u32x4& c0, u32x4& c1) {
  c0 = u32x4{pk(a[0] * sc, a[1] * sc), pk(a[2] * sc, a[3] * sc), pk(a[4] * sc, a[5] * sc), pk(a[6] * sc, a[7] * sc)};
  c1 = u32x4{pk(a[8] * sc, a[9] * sc), pk(a[10] * sc, a[11] * sc), pk(a[12] * sc, a[13] * sc), pk(a[14] * sc, a[15] * sc)};
}
__device__ __forceinline__ float halfmax(float m) {
  auto rr = __builtin_amdgcn_permlane32_swap(__float_as_uint(m), __float_as_uint(m), false, false);
  return __builtin_fmaxf(__uint_as_float(rr[0]), __uint_as_float(rr[1]));
}
__device__ __forceinline__ float max16(const f32x16& s) {
  float a = __builtin_fmaxf(s[0], s[1]);
#pragma unroll
  for (int r = 2; r < 16; ++r) a = __builtin_fmaxf(a, s[r]);
  return a;
}
__device__ __forceinline__ f32x16 splat16(float v) {
  f32x16 r;
#pragma unroll
  for (int i = 0; i < 16; ++i) r[i] = v;
  return r;
}
#define RBIAS 64.0f
constexpr float REF_BIAS = RBIAS;
constexpr float L_LIMIT = 1.099511627776e12f;

__device__ __forceinline__ void attn_safe(const char* Kh, const char* Vh, const char* Qt, int lane, u32x4& c0, u32x4& c1) {
  const rsrc_t rq = mkrs(Qt), rk = mkrs(Kh), rv = mkrs(Vh);
  const bf16x8 q0 = ld_dev(rq, lane * 16), q1 = ld_dev(rq, 1024 + lane * 16);
  float m = -3.0e38f, l = 0.f;
  f32x16 o = {};
  const f32x16 z = {};
#pragma unroll 1
  for (int kt = 0; kt < 64; ++kt) {
    const bf16x8 k0 = ld_dev(rk, lane * 16, kt * 2048), k1 = ld_dev(rk, 1024 + lane * 16, kt * 2048);
    const bf16x8 v0 = ld_dev(rv, lane * 16, kt * 2048), v1 = ld_dev(rv, 1024 + lane * 16, kt * 2048);
    f32x16 s = MFMA32(k0, q0, z);
    s = MFMA32(k1, q1, s);
    const float mn = __builtin_fmaxf(m, halfmax(max16(s)));
    const float al = __builtin_amdgcn_exp2f(m - mn);
    m = mn;
    float rs = 0.f;
#pragma unroll
    for (int r = 0; r < 16; ++r) { s[r] = __builtin_amdgcn_exp2f(s[r] - mn); o[r] *= al; }
    u32x4 p0, p1;
    pack16(s, 1.0f, p0, p1);
#pragma unroll
    for (int r = 0; r < 4; ++r) {
      rs += __uint_as_float(p0[r] << 16) + __uint_as_float(p0[r] & 0xffff0000u);
      rs += __uint_as_float(p1[r] << 16) + __uint_as_float(p1[r] & 0xffff0000u);
    }
    l = l * al + rs;
    o = MFMA32(v0, BC(p0), o);
    o = MFMA32(v1, BC(p1), o);
  }
  auto rr = __builtin_amdgcn_permlane32_swap(__float_as_uint(l), __float_as_uint(l), false, false);
  l = __uint_as_float(rr[0]) + __uint_as_float(rr[1]);
  pack16(o, 1.0f / l, c0, c1);
}

#define EXP2(x) __builtin_amdgcn_exp2f(x)
#define X1(S, i) do { S[i] = EXP2(S[i]); PIN(S[i]); SBAR(); } while (0)
#define XT(T, S, i) do { T = EXP2(S[i]); PIN(T); SBAR(); } while (0)
#define CV(P, j, S, i) do { P[j] = pk(S[i], S[i + 1]); PIN(P[j]); SBAR(); } while (0)
#define CT(P, j, A, B) do { P[j] = pk(A, B); PIN(P[j]); SBAR(); } while (0)
#define SUMP(L, P) asm volatile("v_mfma_f32_16x16x32_bf16 %0, %1, %2, %0" : "+v"(L) : "v"(ones), "v"(P))
#define UNIT(S, Pn0, Pn1, Sn, KA, KB_, Qa, Qb, NM, O, L, VA, VB, Pp0, Pp1, RD0, RD1, X2, X3)     \
  do {                                                                                          \
    Sn = MFMA32(KA, Qa, NM); SBAR();                                                            \
    X1(S, 0); RD0; SBAR(); CT(Pp1, 2, tl0, tl1); X1(S, 1); X1(S, 2);                            \
    Sn = MFMA32(KB_, Qb, Sn); SBAR();                                                           \
    X1(S, 3); RD1; SBAR(); CT(Pp1, 3, tl2, tl3); X1(S, 4); X1(S, 5);                            \
    O = MFMA32(VA, BC(Pp0), O); SBAR();                                                         \
    CV(Pn0, 0, S, 0); X1(S, 6); X2; SBAR(); X1(S, 7); CV(Pn0, 1, S, 2);                         \
    SUMP(L, Pp0); SBAR();                                                                       \
    X1(S, 8); X1(S, 9); CV(Pn0, 2, S, 4);                                                       \
    O = MFMA32(VB, BC(Pp1), O); SBAR();                                                         \
    X1(S, 10); X3; SBAR(); CV(Pn0, 3, S, 6); X1(S, 11); XT(tl0, S, 12);                         \
    SUMP(L, Pp1); SBAR();                                                                       \
    CV(Pn1, 0, S, 8); XT(tl1, S, 13); XT(tl2, S, 14); CV(Pn1, 1, S, 10); XT(tl3, S, 15);        \
  } while (0)
#define RECENTRE(NM, O, L, FE)                                                                  \
  do {                                                                                          \
    int e = __builtin_amdgcn_frexp_expf(L[0]) + (int)REF_BIAS;                                  \
    e = e > 0 ? e : 0;                                                                          \
    FE = (float)e;                                                                              \
    _Pragma("unroll") for (int r = 0; r < 16; ++r) { O[r] = __builtin_amdgcn_ldexpf(O[r], -e); NM[r] -= FE; } \
    _Pragma("unroll") for (int r = 0; r < 4; ++r) L[r] = __builtin_amdgcn_ldexpf(L[r], -e);     \
  } while (0)

constexpr int S_P1 = 0, S_P2 = 16, S_AT = 48, S_MG = 56, S_P4 = 57, S_P5 = 89, S_END = 105;

struct Ctx {
  const char *a1, *b1, *a2, *b2, *kv, *a4, *b4r, *b4i, *a5, *b5;
  unsigned lds0;
  int lw, lane;
};
template <int PH>
struct Loader {
  unsigned va[4], vb[2];
  __device__ __forceinline__ void init(const Ctx& c) {
#pragma unroll
    for (int j = 0; j < 4; ++j)
      va[j] = PH == 1 ? piece_off<OP_ROW>(c.lw * 4 + j, c.lane, 2048) : PH == 2 ? piece_off<OP_ROW>(c.lw * 4 + j, c.lane, 4096)
            : PH == 3 ? c.lane * 16 : PH == 4 ? piece_off<OP_FRAG>(c.lw * 4 + j, c.lane, 65536) : piece_off<OP_FRAG>(c.lw * 4 + j, c.lane, 32768);
#pragma unroll
    for (int j = 0; j < 2; ++j)
      vb[j] = PH == 1 ? piece_off<OP_TR>(c.lw * 2 + j, c.lane, 6144) : PH == 2 ? piece_off<OP_TR>(c.lw * 2 + j, c.lane, 2048)
            : PH == 4 ? piece_off<OP_ROW>(c.lw * 2 + j, c.lane, 2048) : piece_off<OP_TRP>(c.lw * 2 + j, c.lane, 2048);
  }
  __device__ __forceinline__ void piece(const Ctx& c, int t, int s, int j) const {
    const unsigned d = c.lds0 + (s & 3) * SLOT;
    if constexpr (PH == 1) {
      if (j < 4) blds(mkrs(c.a1), va[j], t * 128, d + (c.lw * 4 + j) * 1024);
      else blds(mkrs(c.b1), vb[j & 1], t * (64 * 6144) + ((j - 4) >> 1) * 2048, d + 16384 + ((j - 4) >> 1) * 8192 + (c.lw * 2 + (j & 1)) * 1024);
    } else if constexpr (PH == 2) {
      if (j < 4) blds(mkrs(c.a2), va[j], t * 128, d + (c.lw * 4 + j) * 1024);
      else blds(mkrs(c.b2), vb[j & 1], t * (64 * 2048) + ((j - 4) >> 1) * 128, d + 16384 + ((j - 4) >> 1) * 8192 + (c.lw * 2 + (j & 1)) * 1024);
    } else if constexpr (PH == 3) {
      blds<true>(mkrs(c.kv), va[0], t * 8192 + j * 1024, d + c.lw * 8192 + j * 1024);
    } else if constexpr (PH == 4) {
      if (j < 4) blds<true>(mkrs(c.a4), va[j], t * 2048, d + (c.lw * 4 + j) * 1024);
      else blds(mkrs(t < 16 ? c.b4r : c.b4i), vb[j - 4], (t & 15) * 128, d + 16384 + (c.lw * 2 + j - 4) * 1024);
    } else {
      if (j < 4) blds<true>(mkrs(c.a5), va[j], t * 2048, d + (c.lw * 4 + j) * 1024);
      else blds(mkrs(c.b5), vb[j - 4], t * (64 * 2048), d + 16384 + (c.lw * 2 + j - 4) * 1024);
    }
  }
  static constexpr int NP = PH == 1 ? 10 : PH <= 3 ? 8 : 6;
  __device__ __forceinline__ void stage(const Ctx& c, int t, int s) const {
#pragma unroll
    for (int j = 0; j < NP; ++j) piece(c, t, s, j);
  }
};

template <bool LD>
__device__ __forceinline__ void attention(const Ctx& c, const bf16x8 (&q)[4], const char* Qw, const char* Kh, const char* Vh, rsrc_t rO,
                                          unsigned ooff, int grp, int wq, const char* pU, gu32* uflags, gu32* myflag, unsigned tag, lptr smem) {
  const int lane = c.lane;
  const bf16x8 qa0 = q[0], qb0 = q[1], qa1 = q[2], qb1 = q[3];
  u32x4 ut;
  lptr usp = smem + wq * SLOT + 32768 + lane * 16;
#define URD(n) do { if constexpr (!LD) ut = *(const LAS u32x4*)(usp + (((t & 1) * 4 + (n)) << 10)); } while (0)
#define UST(n) do { if constexpr (!LD) st_wt(mkrs(pU, t < 2), lane * 16 + ((n) & 1) * 1024 + ((t & 1) * 2 + ((n) >> 1)) * 65536, ut); } while (0)
  Loader<3> l3; Loader<4> l4;
  if constexpr (LD) { l3.init(c); l4.init(c); }
  bf16x8 ones;
  {
    const __bf16 v = (((lane >> 2) ^ (lane >> 4)) & 1) ? (__bf16)0.0f : (__bf16)1.0f;
#pragma unroll
    for (int j = 0; j < 8; ++j) ones[j] = v;
  }
#define RDK(dst, P, n) dst = *(const LAS bf16x8*)((P) + (n) * 1024)
#define RDV(dst, P, n) dst = *(const LAS bf16x8*)((P) + 8192 + (n) * 1024)
  bf16x8 k0a, k0b, k1a, k1b, v0a, v0b, v1a, v1b;
  f32x16 sA, sB, nm0, nm1, o0 = {}, o1 = {};
  f32x4 l0 = {}, l1 = {};
  u32x4 pA0, pA1, pB0 = {}, pB1 = {};
  lptr kb = smem + grp * 16384 + lane * 16;
  int cur = (S_AT & 3) * SLOT;
  RDK(k0a, kb + cur, 0); RDK(k0b, kb + cur, 1); RDK(k1a, kb + cur, 2); RDK(k1b, kb + cur, 3);
  RDV(v0a, kb + cur, 0); RDV(v0b, kb + cur, 1);
  {
    const f32x16 z = {};
    f32x16 s0 = MFMA32(k0a, qa0, z); s0 = MFMA32(k0b, qb0, s0);
    f32x16 s1 = MFMA32(k1a, qa0, z); s1 = MFMA32(k1b, qb0, s1);
    nm0 = splat16(-(__builtin_ceilf(halfmax(__builtin_fmaxf(max16(s0), max16(s1)))) + REF_BIAS));
    s0 = MFMA32(k0a, qa1, z); s0 = MFMA32(k0b, qb1, s0);
    s1 = MFMA32(k1a, qa1, z); s1 = MFMA32(k1b, qb1, s1);
    nm1 = splat16(-(__builtin_ceilf(halfmax(__builtin_fmaxf(max16(s0), max16(s1)))) + REF_BIAS));
  }
  PIN(nm0); PIN(nm1);
  sA = MFMA32(k0a, qa0, nm0); sA = MFMA32(k0b, qb0, sA);
  v1a = v0a; v1b = v0a;
  STAMP(4);
  int t = 0;
  float tl0 = 0.f, tl1 = 0.f, tl2 = 0.f, tl3 = 0.f;
#define NOP (void)0
#define UE(KA, KB_, VA, VB, R0, R1, X2, X3) UNIT(sA, pA0, pA1, sB, KA, KB_, qa1, qb1, nm1, o1, l1, VA, VB, pB0, pB1, R0, R1, X2, X3)
#define UO(KA, KB_, VA, VB, R0, R1, X2, X3) UNIT(sB, pB0, pB1, sA, KA, KB_, qa0, qb0, nm0, o0, l0, VA, VB, pA0, pA1, R0, R1, X2, X3)
#pragma unroll 1
  while (t < 8) {
    bool trig;
#pragma unroll 1
    do {
      lptr cp = kb + cur;
      cur = cur == 3 * SLOT ? 0 : cur + SLOT;
      lptr np = kb + cur;
      SBAR();
      UE(k0a, k0b, v1a, v1b, RDK(k1a, cp, 2), RDK(k1b, cp, 3), URD(0), UST(0));
      UO(k1a, k1b, v0a, v0b, RDV(v1a, cp, 2), RDV(v1b, cp, 3), URD(1), UST(1));
      UE(k1a, k1b, v0a, v0b, RDK(k0a, cp, 4), RDK(k0b, cp, 5), URD(2), UST(2));
      UO(k0a, k0b, v1a, v1b, RDV(v0a, cp, 4), RDV(v0b, cp, 5), URD(3), UST(3));
      UE(k0a, k0b, v1a, v1b, RDK(k1a, cp, 6), RDK(k1b, cp, 7), NOP, NOP);
      UO(k1a, k1b, v0a, v0b, RDV(v1a, cp, 6), RDV(v1b, cp, 7), NOP, NOP);
      if constexpr (LD) {
        drain();
        if (t == 5 && wq == 0) await(uflags, 16, tag, lane, smem);
        bar_l();
        if (t < 4) l3.stage(c, t + 4, S_AT + t + 4);
        else if (t > 4) l4.stage(c, t - 5, S_P4 + t - 5);
        else if (wq == 0) mail(uflags, 16, lane, c.lds0);
        if (t == 2) set_flag(myflag, tag);
      } else {
        bar_vl<4>();
      }
      SBAR();
      UE(k1a, k1b, v0a, v0b, RDK(k0a, np, 0), RDK(k0b, np, 1), NOP, NOP);
      UO(k0a, k0b, v1a, v1b, RDV(v0a, np, 0), RDV(v0b, np, 1); trig = __any(l0[0] + l1[0] > L_LIMIT), NOP, NOP);
      ++t;
    } while (t < 8 && !trig);
    if (t >= 8) break;
    asm volatile("s_nop 7" : "+v"(l0), "+v"(l1));
    pB1[2] = pk(tl0, tl1); pB1[3] = pk(tl2, tl3);
    o1 = MFMA32(v1a, BC(pB0), o1); o1 = MFMA32(v1b, BC(pB1), o1);
    l1 = MFMA16(ones, BC(pB0), l1); l1 = MFMA16(ones, BC(pB1), l1);
    pB0 = u32x4{}; pB1 = u32x4{}; tl0 = 0.f; tl1 = 0.f; tl2 = 0.f; tl3 = 0.f;
    float f0, f1;
    RECENTRE(nm0, o0, l0, f0);
    RECENTRE(nm1, o1, l1, f1);
#pragma unroll
    for (int r = 0; r < 16; ++r) sA[r] -= f0;
    asm volatile("s_nop 1" : "+v"(l0), "+v"(l1), "+v"(pB0), "+v"(pB1));
  }
  STAMP(5);
  asm volatile("s_nop 7" : "+v"(l0), "+v"(l1));
  pB1[2] = pk(tl0, tl1); pB1[3] = pk(tl2, tl3);
  o1 = MFMA32(v1a, BC(pB0), o1); o1 = MFMA32(v1b, BC(pB1), o1);
  l1 = MFMA16(ones, BC(pB0), l1); l1 = MFMA16(ones, BC(pB1), l1);
  lptr mg = smem + (S_MG & 3) * SLOT + lane * 16;
  {
    const f32x16& og = LD ? o0 : o1;
    lptr d = mg + ((1 - grp) * 4 + wq) * 5120;
#pragma unroll
    for (int q = 0; q < 4; ++q) *(LAS f32x4*)(d + q * 1024) = f32x4{og[4 * q], og[4 * q + 1], og[4 * q + 2], og[4 * q + 3]};
    *(LAS f32x4*)(d + 4096) = f32x4{LD ? l0[0] : l1[0], LD ? nm0[0] : nm1[0], 0.f, 0.f};
  }
  bar_l();
  f32x16 o = LD ? o1 : o0;
  float l = LD ? l1[0] : l0[0];
  {
    lptr d = mg + (grp * 4 + wq) * 5120;
    const f32x4 e = *(const LAS f32x4*)(d + 4096);
    const float nmk = LD ? nm1[0] : nm0[0], nmin = __builtin_fminf(nmk, e[1]);
    const int ek = (int)__builtin_fmaxf(nmin - nmk, -400.f), eo = (int)__builtin_fmaxf(nmin - e[1], -400.f);
    l = __builtin_amdgcn_ldexpf(l, ek) + __builtin_amdgcn_ldexpf(e[0], eo);
#pragma unroll
    for (int q = 0; q < 4; ++q) {
      const f32x4 x = *(const LAS f32x4*)(d + q * 1024);
#pragma unroll
      for (int r = 0; r < 4; ++r) o[4 * q + r] = __builtin_amdgcn_ldexpf(o[4 * q + r], ek) + __builtin_amdgcn_ldexpf(x[r], eo);
    }
  }
  float chk = l;
#pragma unroll
  for (int r = 0; r < 16; ++r) chk += __builtin_fabsf(o[r]);
  u32x4 x, y;
  if (__builtin_expect(__any(!(chk < 3.0e38f) || !(l > 0.f)), 0)) attn_safe(Kh, Vh, Qw + grp * 2048, lane, x, y);
  else pack16(o, 1.0f / l, x, y);
  const unsigned vo = ooff + grp * 65536 + ((lane >> 4) & 1) * 32768 + (lane >> 5) * 256 + (lane & 15) * 16;
  st_wt(rO, vo, x); st_wt(rO, vo + 512, y);
}

__device__ __forceinline__ void store_out(const f32x4 (&acc)[4][2], u16* out, int row0, int col0, int lane) {
#pragma unroll
  for (int mi = 0; mi < 4; ++mi)
    *(u32x4*)(out + (row0 + mi * 16 + (lane & 15)) * 2048 + col0 + (lane >> 4) * 8) = natural(pack2(acc[mi][0], acc[mi][1], 1.0f));
}

__global__ __launch_bounds__(512, 2) void fused(const u16* __restrict__ xA, const u16* __restrict__ Wsr, const u16* __restrict__ Wsi,
                                                const u16* __restrict__ Wdr, const u16* __restrict__ Wdi, const u16* __restrict__ xB,
                                                const u16* __restrict__ wqkv, const u16* __restrict__ wo, u16* __restrict__ out,
                                                char* ws) {
  extern __shared__ __attribute__((aligned(16))) char smem_[];
  lptr smem = (lptr)smem_;
  const int lane = threadIdx.x & 63, w = __builtin_amdgcn_readfirstlane(threadIdx.x >> 6), wm = (w >> 1) & 1, wn = w & 1;
  const int xcd = blockIdx.x & 7, i = blockIdx.x >> 3;
  gu32* flags = (gu32*)ws + 64;
  char* QKV = ws + (1 << 20);
  char* Of = QKV + (12 << 20);
  char* Uf = Of + (4 << 20);
  const int j2 = xcd * 2 + (i >> 4), tb1 = i & 15;
  const int tbA = xcd * 2 + (i >> 4), im = (i >> 3) & 1, nb2 = i & 7, eb = i & 15;
  const int head = xcd * 4 + (i >> 3), qb = i & 7;
  const int tb5 = (xcd >> 1) * 4 + (i & 3), nb5 = (xcd & 1) * 8 + (i >> 2);
  const char* Kh = QKV + (4 << 20) + head * 131072;
  const char* Vh = QKV + (8 << 20) + head * 131072;
  const int qt = qb * 8 + (w & 3) * 2;
  const char* Qw = QKV + (head * 64 + qt) * 2048;
  const unsigned ooff = qt * 65536 + head * 1024;
  Ctx c;
  c.lane = lane; c.lw = w & 3; c.lds0 = (unsigned)(unsigned long)smem;
  STAMP(0);
  if (w >= 4) {
    c.a1 = (const char*)(xB + tb1 * 128 * D_); c.b1 = (const char*)(wqkv + j2 * 64);
    c.a2 = (const char*)((im ? Wsi : Wsr) + tbA * 128 * S_); c.b2 = (const char*)(xA + nb2 * 128);
    c.kv = ((w & 1) ? Vh : Kh) + ((w >> 1) & 1) * 65536;
    c.a4 = Uf + tbA * 8 * 65536; c.b4r = (const char*)(Wdr + eb * 64 * D_); c.b4i = (const char*)(Wdi + eb * 64 * D_);
    c.a5 = Of + tb5 * 8 * 32768; c.b5 = (const char*)(wo + nb5 * 64);
    unsigned tag;
    {
      Loader<1> l1; l1.init(c);
#pragma unroll
      for (int s = 0; s < 4; ++s) l1.stage(c, s, s);
      asm volatile("s_load_dword %0, %1, 0x0\n\ts_waitcnt lgkmcnt(0)" : "=s"(tag) : "s"(ws) : "memory");
      tag = tag * 1664525u + 1013904223u;
      bar_v<30>();
#pragma unroll 1
      for (int s = 0; s < 12; ++s) { bar_v<20>(); l1.stage(c, s + 4, s + 4); }
    }
    gu32 *fq = flags + (head >> 1) * 16, *fo = flags + 512 + (tb5 >> 1) * 32;
    {
      Loader<2> l2; l2.init(c);
      const char* pKVo = QKV + (4 << 20) + ((j2 * 2 + (w & 1)) * 64 + tb1 * 4 + ((w >> 1) & 1) * 2) * 2048;
#pragma unroll 1
      for (int s = 12; s < 44; ++s) {
        bar_v<16>();
        if (s == 40 && w == 4) mail(fq, 16, lane, c.lds0);
        l2.stage(c, s - 12, s + 4);
        if (s >= 16 && s < 24) {
          const int n = s - 16;
          const u32x4 v = *(const LAS u32x4*)(smem + c.lw * SLOT + 32768 + n * 1024 + lane * 16);
          __builtin_amdgcn_raw_buffer_store_b128(v, mkrs(pKVo), lane * 16, (n >> 2) * (4 << 20) + (n & 3) * 1024, 16);
        }
        if (s == 27) set_flag(flags + j2 * 16 + tb1, tag);
      }
      if (w == 4) await(fq, 16, tag, lane, smem);
    }
    bf16x8 q[4];
    {
      Loader<3> l3; l3.init(c);
      bar_v<16>();
#pragma unroll
      for (int k = 0; k < 4; ++k) blds<true>(mkrs(Qw), lane * 16, k * 1024, c.lds0 + c.lw * SLOT + 32768 + k * 1024);
      l3.stage(c, 0, 48);
#pragma unroll 1
      for (int s = 45; s < 47; ++s) { bar_v<16>(); l3.stage(c, s - 44, s + 4); }
      asm volatile("s_waitcnt vmcnt(16)" ::: "memory");
#pragma unroll
      for (int k = 0; k < 4; ++k) q[k] = *(const LAS bf16x8*)(smem + c.lw * SLOT + 32768 + k * 1024 + lane * 16);
      bar_l();
      l3.stage(c, 3, 51);
    }
    STAMP(3);
    attention<true>(c, q, Qw, Kh, Vh, mkrs(Of), ooff, 1, w & 3, nullptr, flags + 1024 + tbA * 16, flags + 1024 + tbA * 16 + (i & 15), tag, smem);
    STAMP(6);
    {
      Loader<4> l4; l4.init(c);
#pragma unroll 1
      for (int s = S_MG; s < S_MG + 3; ++s) { bar_vl<14>(); l4.stage(c, s + 4 - S_P4, s + 4); }
#pragma unroll 1
      for (int s = S_MG + 3; s < S_P5 - 4; ++s) {
        bar_v<12>();
        if (s == S_P5 - 8 && w == 4) mail(fo, 32, lane, c.lds0);
        l4.stage(c, s + 4 - S_P4, s + 4);
        if (s == S_P4 + 4) set_flag(flags + 512 + qb * 32 + head, tag);
      }
      if (w == 4) await(fo, 32, tag, lane, smem);
    }
    {
      Loader<5> l5; l5.init(c);
#pragma unroll 1
      for (int s = S_P5 - 4; s < S_END - 4; ++s) { bar_v<12>(); l5.stage(c, s + 4 - S_P5, s + 4); }
      bar_v<12>(); bar_v<6>(); bar_v<0>(); bar_v<0>();
    }
    if (blockIdx.x == 0 && w == 4) {
#pragma unroll 1
      for (unsigned spins = 0; spins < 400000u; ++spins) {
        const u32x4 f = __builtin_amdgcn_raw_buffer_load_b128(mkrs((const void*)flags), lane * 16, 0, 16);
        if (__all(f[0] == tag && f[1] == tag && f[2] == tag && f[3] == tag)) break;
        __builtin_amdgcn_s_sleep(2);
      }
      set_flag((gu32*)ws, tag);
    }
    return;
  }
  const unsigned lds0 = c.lds0;
  lptr park = smem + w * SLOT + 32768;
  bar_l();
  {
    FragRd<OP_ROW, 1> ra; ra.init(wm * 4, lane, lds0 + (S_P1 & 3) * SLOT);
    FragRd<OP_TR, 2> rb; rb.init(wn * 2, lane, lds0 + (S_P1 & 3) * SLOT + 16384);
    f32x4 acc[4][6] = {};
    consume<4, 6, 4, S_P1, 16, 0>(acc, ra, rb);
    STAMP(1);
    const rsrc_t rQ = mkrs(QKV + ((j2 * 2 + wn) * 64 + tb1 * 4 + wm * 2) * 2048);
    const int cc = lane & 15, g = lane >> 4;
#pragma unroll
    for (int mi = 0; mi < 4; ++mi) {
      const unsigned blk = (mi >> 1) * 2048 + g * 512;
      st_wt(rQ, blk + ((mi & 1) * 16 + cc) * 16, pack2(acc[mi][0], acc[mi][1], 0.17677669529663687f * 1.4426950408889634f));
      *(LAS u32x4*)(park + blk + ((cc >> 3) * 16 + (mi & 1) * 8 + (cc & 7)) * 16) = pack2(acc[mi][2], acc[mi][3], 1.0f);
      *(LAS u32x4*)(park + 4096 + blk + ((mi & 1) * 16 + cc) * 16) = pack2(acc[mi & 2][4 + (mi & 1)], acc[(mi & 2) + 1][4 + (mi & 1)], 1.0f);
    }
    STAMP(2);
  }
  bf16x8 q[4];
  {
    FragRd<OP_ROW, 1> ra; ra.init(wm * 4, lane, lds0 + (S_P2 & 3) * SLOT);
    FragRd<OP_TR, 4> rb; rb.init(0, lane, lds0 + (S_P2 & 3) * SLOT + 16384 + wn * 8192);
    f32x4 acc[4][4] = {};
    consume<4, 4, 4, S_P2, 32, 11>(acc, ra, rb);
    STAMP(3);
#pragma unroll
    for (int k = 0; k < 4; ++k) q[k] = *(const LAS bf16x8*)(park + k * 1024 + lane * 16);
    const float sc = im ? -1.0f : 1.0f;
#pragma unroll
    for (int mi = 0; mi < 4; ++mi)
#pragma unroll
      for (int n2 = 0; n2 < 2; ++n2)
        *(LAS u32x4*)(park + (mi * 2 + n2) * 1024 + lane * 16) = natural(pack2(acc[mi][2 * n2], acc[mi][2 * n2 + 1], sc));
  }
  attention<false>(c, q, Qw, Kh, Vh, mkrs(Of), ooff, 0, w, Uf + (tbA * 8 + wm * 4) * 65536 + (im * 32 + nb2 * 4 + wn * 2) * 1024,
                   nullptr, nullptr, 0u, smem);
  STAMP(6);
  bar_l();
  {
    FragRd<OP_FRAG, 1> ra; ra.init(wm * 4, lane, lds0 + (S_P4 & 3) * SLOT);
    FragRd<OP_ROW, 1> rb; rb.init(wn * 2, lane, lds0 + (S_P4 & 3) * SLOT + 16384);
    f32x4 acc[4][2] = {};
    consume<4, 2, 2, S_P4, 32, 4>(acc, ra, rb);
    STAMP(7);
    store_out(acc, out, tbA * 128 + wm * 64, eb * 64 + wn * 32, lane);
  }
  {
    FragRd<OP_FRAG, 1> ra; ra.init(wm * 4, lane, lds0 + (S_P5 & 3) * SLOT);
    FragRd<OP_TRP, 2> rb; rb.init(wn * 2, lane, lds0 + (S_P5 & 3) * SLOT + 16384);
    f32x4 acc[4][2] = {};
    consume<4, 2, 2, S_P5, 16, 0>(acc, ra, rb);
    STAMP(8);
    store_out(acc, out, tb5 * 128 + wm * 64, 1024 + nb5 * 64 + wn * 32, lane);
  }
}

extern "C" void kernel_launch(void* const* d_in, const int* in_sizes, int n_in, void* d_out, int out_size, void* d_ws,
                              size_t ws_size, hipStream_t stream) {
  (void)hipFuncSetAttribute((const void*)fused, hipFuncAttributeMaxDynamicSharedMemorySize, LDS_BYTES);
  fused<<<256, 512, LDS_BYTES, stream>>>((const u16*)d_in[0], (const u16*)d_in[1], (const u16*)d_in[2], (const u16*)d_in[3],
                                         (const u16*)d_in[4], (const u16*)d_in[5], (const u16*)d_in[6], (const u16*)d_in[7],
                                         (u16*)d_out, (char*)d_ws);
}
```

```cpp
#include <hip/hip_runtime.h>
#include <hip/hip_bf16.h>
#include <type_traits>

typedef unsigned short u16;
typedef __attribute__((ext_vector_type(8))) __bf16 bf16x8;
typedef __attribute__((ext_vector_type(2))) __bf16 bf16x2;
typedef __attribute__((ext_vector_type(2))) float f32x2;
typedef __attribute__((ext_vector_type(4))) float f32x4;
typedef __attribute__((ext_vector_type(16))) float f32x16;
typedef __attribute__((ext_vector_type(4))) unsigned u32x4;
typedef __attribute__((ext_vector_type(2))) unsigned u32x2;
typedef __attribute__((ext_vector_type(4))) short s16x4;
#define LAS __attribute__((address_space(3)))
#define GAS __attribute__((address_space(1)))
typedef LAS char* lptr;
typedef GAS unsigned gu32;
typedef __amdgpu_buffer_rsrc_t rsrc_t;

#define SBAR() __builtin_amdgcn_sched_barrier(0)
#define PIN(x) asm volatile("" : "+v"(x))
#define MFMA32(a, b, c) __builtin_amdgcn_mfma_f32_32x32x16_bf16((a), (b), (c), 0, 0, 0)
#define MFMA16(a, b, c) __builtin_amdgcn_mfma_f32_16x16x32_bf16((a), (b), (c), 0, 0, 0)
#define BC(x) __builtin_bit_cast(bf16x8, x)
#define RLX_AGENT __ATOMIC_RELAXED, __HIP_MEMORY_SCOPE_AGENT
template <int N> __device__ __forceinline__ void bar_vl() { asm volatile("s_waitcnt vmcnt(%0) lgkmcnt(0)\n\ts_barrier" ::"n"(N) : "memory"); }

#define STAMP(I)
#define ST2(W, I)

#define PF0 1
#define DNT 0
#define PWS 2
#define PWI 3
#define PFS 2
#define PFI 3
constexpr int S_ = 2048, D_ = 1024, SLOT = 40960, LDS_BYTES = 4 * SLOT;
enum { OP_ROW = 0, OP_FRAG = 1, OP_TR = 2, OP_TRP = 3 };

__device__ __forceinline__ unsigned pk(float lo, float hi) {
  f32x2 v = {lo, hi};
  return __builtin_bit_cast(unsigned, __builtin_convertvector(v, bf16x2));
}
__device__ __forceinline__ rsrc_t mkrs(const void* p, bool on = true) {
  return __builtin_amdgcn_make_buffer_rsrc((void*)p, 0, on ? 0x7fffffff : 0, 0x00020000);
}
template <int POL = 0>
__device__ __forceinline__ void blds(rsrc_t r, unsigned voff, unsigned soff, unsigned l) {
  if constexpr (POL == 1) asm volatile("s_mov_b32 m0, %0\n\ts_nop 0\n\tbuffer_load_dwordx4 %1, %2, %3 offen sc1 lds" ::"s"(l), "v"(voff), "s"(r), "s"(soff) : "memory");
  else if constexpr (POL == 2) asm volatile("s_mov_b32 m0, %0\n\ts_nop 0\n\tbuffer_load_dwordx4 %1, %2, %3 offen nt lds" ::"s"(l), "v"(voff), "s"(r), "s"(soff) : "memory");
  else asm volatile("s_mov_b32 m0, %0\n\ts_nop 0\n\tbuffer_load_dwordx4 %1, %2, %3 offen lds" ::"s"(l), "v"(voff), "s"(r), "s"(soff) : "memory");
}
__device__ __forceinline__ bf16x8 ld_dev(rsrc_t r, unsigned voff, unsigned soff = 0) {
  return __builtin_bit_cast(bf16x8, __builtin_amdgcn_raw_buffer_load_b128(r, voff, soff, 16));
}
__device__ __forceinline__ void st_wt(rsrc_t r, unsigned voff, u32x4 v) { __builtin_amdgcn_raw_buffer_store_b128(v, r, voff, 0, 16); }

template <int KIND>
__device__ __forceinline__ unsigned piece_off(int p, int i, int ld) {
  if constexpr (KIND == OP_ROW) return (8 * p + (i >> 3)) * ld + (((i & 7) ^ (((p & 1) << 2) | (i >> 4))) << 4);
  else if constexpr (KIND == OP_FRAG) return (p >> 1) * ld + (p & 1) * 1024 + i * 16;
  else if constexpr (KIND == OP_TR) return (8 * p + (i >> 3)) * ld + (((i & 7) ^ ((((i >> 4) & 1) | ((p & 1) << 1)) << 1)) << 4);
  else return (8 * p + (i >> 3)) * ld + (((i & 7) ^ ((i >> 4) << 1)) << 4);
}
template <int KIND, int NBASE>
struct FragRd {
  static constexpr int NB = KIND == OP_ROW ? 2 : (KIND == OP_FRAG ? 1 : NBASE);
  static constexpr int OPS = KIND >= OP_TR ? 2 : 1;
  unsigned base[NB];
  __device__ __forceinline__ void init(int b0, int lane, unsigned img) {
    const int r = lane & 15, g = lane >> 4;
    if constexpr (KIND == OP_ROW) {
#pragma unroll
      for (int ks = 0; ks < 2; ++ks) base[ks] = img + (b0 * 16 + r) * 128 + (((4 * ks + g) ^ ((r >> 1) & 7)) << 4);
    } else if constexpr (KIND == OP_FRAG) {
      base[0] = img + b0 * 2048 + lane * 16;
    } else {
      const int q = r >> 2, p = lane & 3, f = ((q >> 1) & 1) | ((g & 1) << 1);
      const int row = (KIND == OP_TR ? 8 * g : 16 * (g >> 1) + 4 * (g & 1)) + q;
#pragma unroll
      for (int b = 0; b < NBASE; ++b) base[b] = img + row * 128 + (((((b0 + b) ^ f) << 1) | (p >> 1)) << 4) + (p & 1) * 8;
    }
  }
  __device__ __forceinline__ void step(int k, int d) { base[k] += d; PIN(base[k]); }
  __device__ __forceinline__ void rd(u32x4& f, int b, int ks, int half) const {
    if constexpr (KIND == OP_ROW) f = *(const LAS u32x4*)(unsigned long)(base[ks] + b * 2048);
    else if constexpr (KIND == OP_FRAG) f = *(const LAS u32x4*)(unsigned long)(base[0] + b * 2048 + ks * 1024);
    else {
      const u32x2 x = __builtin_bit_cast(u32x2, __builtin_amdgcn_ds_read_tr16_b64_v4i16(
          (LAS s16x4*)(unsigned long)(base[b % NBASE] + (b / NBASE) * 8192 + ks * 4096 + half * (KIND == OP_TR ? 512 : 1024))));
      f[2 * half] = x[0]; f[2 * half + 1] = x[1];
    }
  }
};

__device__ __forceinline__ u32x4 pack2(const f32x4& x, const f32x4& y, float sc) {
  return u32x4{pk(x[0] * sc, x[1] * sc), pk(x[2] * sc, x[3] * sc), pk(y[0] * sc, y[1] * sc), pk(y[2] * sc, y[3] * sc)};
}
__device__ __forceinline__ u32x4 natural(u32x4 c) {
  auto x = __builtin_amdgcn_permlane32_swap(c[0], c[2], false, false);
  auto y = __builtin_amdgcn_permlane32_swap(c[1], c[3], false, false);
  auto p = __builtin_amdgcn_permlane16_swap(x[0], x[1], false, false);
  auto q = __builtin_amdgcn_permlane16_swap(y[0], y[1], false, false);
  return u32x4{p[0], q[0], p[1], q[1]};
}

__device__ __forceinline__ void bar_l() { asm volatile("s_waitcnt lgkmcnt(0)\n\ts_barrier" ::: "memory"); }
template <int N> __device__ __forceinline__ void bar_v() { asm volatile("s_waitcnt vmcnt(%0)\n\ts_barrier" ::"n"(N) : "memory"); }
__device__ __forceinline__ void drain() { asm volatile("s_waitcnt vmcnt(0)" ::: "memory"); }
__device__ __forceinline__ void set_flag(gu32* flag, unsigned tag) {
  if (threadIdx.x == 384) __hip_atomic_store(flag, tag, RLX_AGENT);
}
constexpr int MAIL = 32768 + 4096;
__device__ __forceinline__ void mail(gu32* flags, int n, int lane, unsigned lds0) {
  asm volatile("s_mov_b32 m0, %0\n\ts_nop 0\n\tbuffer_load_dword %1, %2, 0 offen sc1 lds"
               ::"s"(lds0 + MAIL), "v"((lane < n ? lane : 0) * 4), "s"(mkrs((const void*)flags)) : "memory");
}
__device__ __forceinline__ void await(gu32* flags, int n, unsigned tag, int lane, lptr smem) {
  if (__builtin_expect(!__all(*(volatile LAS unsigned*)(smem + MAIL + lane * 4) == tag), 0)) {
#pragma unroll 1
    for (unsigned spins = 0; spins < 400000u; ++spins) {
      if (__all(__hip_atomic_load(flags + (lane < n ? lane : 0), RLX_AGENT) == tag)) break;
      __builtin_amdgcn_s_sleep(2);
    }
  }
  __builtin_amdgcn_fence(__ATOMIC_ACQUIRE, "wavefront");
}
#define WAIT_LGKM(n) __builtin_amdgcn_s_waitcnt(0xC07F | ((n) << 8))

template <int MI, int NI, int NSWAP, int S0, int NT, int ND, class RA, class RB>
__device__ __forceinline__ void consume(f32x4 (&acc)[MI][NI], RA& ra, RB& rb) {
  u32x4 a0[MI], b0[NI], a1[MI], b1[NI];
  constexpr int NM = MI * NI, NBO = NI * RB::OPS, NR = MI + NBO, NU = RA::NB + RB::NB;
  auto rdop = [&](u32x4(&na)[MI], u32x4(&nb)[NI], int ks, int z) {
    if (z == 0) ra.rd(na[0], 0, ks, 0);
    else if (z <= NBO) rb.rd(nb[(z - 1) / RB::OPS], (z - 1) / RB::OPS, ks, (z - 1) % RB::OPS);
    else ra.rd(na[z - NBO], z - NBO, ks, 0);
  };
  auto mma = [&](const u32x4(&fa)[MI], const u32x4(&fb)[NI], int i) {
    const int mi = i / NI, ni = i % NI;
    acc[mi][ni] = ni < NSWAP ? MFMA16(BC(fb[ni]), BC(fa[mi]), acc[mi][ni]) : MFMA16(BC(fa[mi]), BC(fb[ni]), acc[mi][ni]);
    SBAR();
  };
#pragma unroll
  for (int z = 0; z < NR; ++z) { rdop(a0, b0, 0, z); SBAR(); }
  int slot = S0 & 3;
#pragma unroll 1
  for (int seg = ND ? 0 : 1; seg < 2; ++seg) {
#pragma unroll 1
    for (int t = seg ? NT - ND : ND; t > 0; --t) {
      slot = (slot + 1) & 3;
      const int d = slot == 0 ? -3 * SLOT : SLOT;
      WAIT_LGKM(MI - 1);
      SBAR();
#pragma unroll
      for (int i = 0; i < NM + 2; ++i) {
        if (i < NM) mma(a0, b0, i);
        else { if (i == NM) { WAIT_LGKM(0); SBAR(); } mma(a1, b1, i - NM); }
        if (i < NR) rdop(a1, b1, 1, i);
        else {
          constexpr int G = NM + 2 - NR;
#pragma unroll
          for (int u = (i - NR) * NU / G; u < (i - NR + 1) * NU / G; ++u) { if (u < RA::NB) ra.step(u, d); else rb.step(u - RA::NB, d); }
        }
        SBAR();
      }
      asm volatile("s_barrier" ::: "memory");
      SBAR();
      constexpr int G1 = NM - 2;
#pragma unroll
      for (int i = 0; i < G1; ++i) {
        if (NR > G1 && i < NR - G1) { rdop(a0, b0, 0, 2 * i); SBAR(); }
        mma(a1, b1, i + 2);
        const int z = NR > G1 ? (i < NR - G1 ? 2 * i + 1 : i + NR - G1) : i;
        if (z < NR) rdop(a0, b0, 0, z);
        SBAR();
      }
    }
    if (ND) drain();
  }
}

__device__ __forceinline__ void pack16(const f32x16& a, float sc, u32x4& c0, u32x4& c1) {
  c0 = u32x4{pk(a[0] * sc, a[1] * sc), pk(a[2] * sc, a[3] * sc), pk(a[4] * sc, a[5] * sc), pk(a[6] * sc, a[7] * sc)};
  c1 = u32x4{pk(a[8] * sc, a[9] * sc), pk(a[10] * sc, a[11] * sc), pk(a[12] * sc, a[13] * sc), pk(a[14] * sc, a[15] * sc)};
}
__device__ __forceinline__ float halfmax(float m) {
  auto rr = __builtin_amdgcn_permlane32_swap(__float_as_uint(m), __float_as_uint(m), false, false);
  return __builtin_fmaxf(__uint_as_float(rr[0]), __uint_as_float(rr[1]));
}
__device__ __forceinline__ float max16(const f32x16& s) {
  float a = __builtin_fmaxf(s[0], s[1]);
#pragma unroll
  for (int r = 2; r < 16; ++r) a = __builtin_fmaxf(a, s[r]);
  return a;
}
__device__ __forceinline__ f32x16 splat16(float v) {
  f32x16 r;
#pragma unroll
  for (int i = 0; i < 16; ++i) r[i] = v;
  return r;
}
#define RBIAS 64.0f
constexpr float REF_BIAS = RBIAS;
constexpr float L_LIMIT = 1.099511627776e12f;

__device__ __forceinline__ void attn_safe(const char* Kh, const char* Vh, const char* Qt, int lane, u32x4& c0, u32x4& c1) {
  const rsrc_t rq = mkrs(Qt), rk = mkrs(Kh), rv = mkrs(Vh);
  const bf16x8 q0 = ld_dev(rq, lane * 16), q1 = ld_dev(rq, 1024 + lane * 16);
  float m = -3.0e38f, l = 0.f;
  f32x16 o = {};
  const f32x16 z = {};
#pragma unroll 1
  for (int kt = 0; kt < 64; ++kt) {
    const bf16x8 k0 = ld_dev(rk, lane * 16, kt * 2048), k1 = ld_dev(rk, 1024 + lane * 16, kt * 2048);
    const bf16x8 v0 = ld_dev(rv, lane * 16, kt * 2048), v1 = ld_dev(rv, 1024 + lane * 16, kt * 2048);
    f32x16 s = MFMA32(k0, q0, z);
    s = MFMA32(k1, q1, s);
    const float mn = __builtin_fmaxf(m, halfmax(max16(s)));
    const float al = __builtin_amdgcn_exp2f(m - mn);
    m = mn;
    float rs = 0.f;
#pragma unroll
    for (int r = 0; r < 16; ++r) { s[r] = __builtin_amdgcn_exp2f(s[r] - mn); o[r] *= al; }
    u32x4 p0, p1;
    pack16(s, 1.0f, p0, p1);
#pragma unroll
    for (int r = 0; r < 4; ++r) {
      rs += __uint_as_float(p0[r] << 16) + __uint_as_float(p0[r] & 0xffff0000u);
      rs += __uint_as_float(p1[r] << 16) + __uint_as_float(p1[r] & 0xffff0000u);
    }
    l = l * al + rs;
    o = MFMA32(v0, BC(p0), o);
    o = MFMA32(v1, BC(p1), o);
  }
  auto rr = __builtin_amdgcn_permlane32_swap(__float_as_uint(l), __float_as_uint(l), false, false);
  l = __uint_as_float(rr[0]) + __uint_as_float(rr[1]);
  pack16(o, 1.0f / l, c0, c1);
}

#define EXP2(x) __builtin_amdgcn_exp2f(x)
#define X1(S, i) do { S[i] = EXP2(S[i]); PIN(S[i]); SBAR(); } while (0)
#define XT(T, S, i) do { T = EXP2(S[i]); PIN(T); SBAR(); } while (0)
#define CV(P, j, S, i) do { P[j] = pk(S[i], S[i + 1]); PIN(P[j]); SBAR(); } while (0)
#define CT(P, j, A, B) do { P[j] = pk(A, B); PIN(P[j]); SBAR(); } while (0)
#define SUMP(L, P) asm volatile("v_mfma_f32_16x16x32_bf16 %0, %1, %2, %0" : "+v"(L) : "v"(ones), "v"(P))
#define UNIT(S, Pn0, Pn1, Sn, KA, KB_, Qa, Qb, NM, O, L, VA, VB, Pp0, Pp1, RD0, RD1, X2, X3)     \
  do {                                                                                          \
    Sn = MFMA32(KA, Qa, NM); SBAR();                                                            \
    X1(S, 0); RD0; SBAR(); CT(Pp1, 2, tl0, tl1); X1(S, 1); X1(S, 2);                            \
    Sn = MFMA32(KB_, Qb, Sn); SBAR();                                                           \
    X1(S, 3); RD1; SBAR(); CT(Pp1, 3, tl2, tl3); X1(S, 4); X1(S, 5);                            \
    O = MFMA32(VA, BC(Pp0), O); SBAR();                                                         \
    CV(Pn0, 0, S, 0); X1(S, 6); X2; SBAR(); X1(S, 7); CV(Pn0, 1, S, 2);                         \
    SUMP(L, Pp0); SBAR();                                                                       \
    X1(S, 8); X1(S, 9); CV(Pn0, 2, S, 4);                                                       \
    O = MFMA32(VB, BC(Pp1), O); SBAR();                                                         \
    X1(S, 10); X3; SBAR(); CV(Pn0, 3, S, 6); X1(S, 11); XT(tl0, S, 12);                         \
    SUMP(L, Pp1); SBAR();                                                                       \
    CV(Pn1, 0, S, 8); XT(tl1, S, 13); XT(tl2, S, 14); CV(Pn1, 1, S, 10); XT(tl3, S, 15);        \
  } while (0)
#define RECENTRE(NM, O, L, FE)                                                                  \
  do {                                                                                          \
    int e = __builtin_amdgcn_frexp_expf(L[0]) + (int)REF_BIAS;                                  \
    e = e > 0 ? e : 0;                                                                          \
    FE = (float)e;                                                                              \
    _Pragma("unroll") for (int r = 0; r < 16; ++r) { O[r] = __builtin_amdgcn_ldexpf(O[r], -e); NM[r] -= FE; } \
    _Pragma("unroll") for (int r = 0; r < 4; ++r) L[r] = __builtin_amdgcn_ldexpf(L[r], -e);     \
  } while (0)

constexpr int S_P1 = 0, S_P2 = 16, S_AT = 48, S_MG = 56, S_P4 = 57, S_P5 = 89, S_END = 105;

struct Ctx {
  const char *a1, *b1, *a2, *b2, *kv, *a4, *b4r, *b4i, *a5, *b5;
  unsigned lds0;
  int lw, lane;
};
template <int PH, int NW>
struct Loader {
  static constexpr int KA = PH <= 2 ? OP_ROW : OP_FRAG, KB = PH <= 2 ? OP_TR : PH == 4 ? OP_ROW : OP_TRP;
  static constexpr int LDA = PH == 1 ? 2048 : PH == 2 ? 4096 : PH == 4 ? 65536 : 32768, LDB = PH == 1 ? 6144 : 2048;
  static constexpr int NSUB = PH == 1 ? 3 : PH == 2 ? 2 : 1, SUBOFF = PH == 1 ? 2048 : 128;
  static constexpr int PA = 16 / NW, PB = 8 / NW;
  unsigned va[2], vb[2];
  __device__ __forceinline__ void init(const Ctx& c) {
#pragma unroll
    for (int j = 0; j < 2; ++j) {
      va[j] = PH == 3 ? c.lane * 16 : piece_off<KA>(c.lw * PA + j, c.lane, LDA);
      vb[j] = piece_off<KB>(c.lw * PB + j, c.lane, LDB);
    }
  }
  __device__ __forceinline__ void stage(const Ctx& c, int t, int s) const {
    const unsigned d = c.lds0 + (s & 3) * SLOT;
    if constexpr (PH == 3) {
#pragma unroll
      for (int h = 0; h < 4 / NW; ++h) {
        const int part = c.lw * (4 / NW) + h;
#pragma unroll
        for (int j = 0; j < 8; ++j)
          blds<1>(mkrs(c.kv), va[0], (part & 1) * (4 << 20) + (part >> 1) * 65536 + t * 8192 + j * 1024, d + part * 8192 + j * 1024);
      }
    } else {
      const char* pa = PH == 1 ? c.a1 : PH == 2 ? c.a2 : PH == 4 ? c.a4 : c.a5;
      const char* pb = PH == 1 ? c.b1 : PH == 2 ? c.b2 : PH == 4 ? (t < 16 ? c.b4r : c.b4i) : c.b5;
      const int sa = t * (KA == OP_ROW ? 128 : 2048), sb = PH == 4 ? (t & 15) * 128 : t * 64 * LDB;
#pragma unroll
      for (int j = 0; j < PA; ++j)
        blds<(PH >= 4 ? 1 : PH == 2 ? DNT : 0)>(mkrs(pa), va[j & 1], sa + (j >> 1) * (KA == OP_ROW ? 16 * LDA : LDA), d + (c.lw * PA + j) * 1024);
#pragma unroll
      for (int u = 0; u < NSUB; ++u)
#pragma unroll
        for (int j = 0; j < PB; ++j)
          blds<(PH == 1 ? DNT : 0)>(mkrs(pb), vb[j & 1], sb + u * SUBOFF + (j >> 1) * 16 * LDB, d + 16384 + u * 8192 + (c.lw * PB + j) * 1024);
    }
  }
};
__device__ __forceinline__ void touch(const void* p, unsigned voff, unsigned soff) {
  asm volatile("buffer_load_dword v255, %0, %1, %2 offen" ::"v"(voff), "s"(mkrs(p)), "s"(soff) : "memory", "v255");
}

template <bool LD>
__device__ __forceinline__ void attention(const Ctx& c, const bf16x8 (&q)[4], const char* Qw, const char* Kh, const char* Vh, rsrc_t rO,
                                          unsigned ooff, int grp, int wq, const char* pU, gu32* uflags, gu32* myflag, unsigned tag, lptr smem) {
  const int lane = c.lane;
  const bf16x8 qa0 = q[0], qb0 = q[1], qa1 = q[2], qb1 = q[3];
  u32x4 ut;
  lptr usp = smem + wq * SLOT + 32768 + lane * 16;
#define URD(n) do { if constexpr (!LD) ut = *(const LAS u32x4*)(usp + (((t & 1) * 4 + (n)) << 10)); } while (0)
#define UST(n) do { if constexpr (!LD) st_wt(mkrs(pU, t < 2), lane * 16 + ((n) & 1) * 1024 + ((t & 1) * 2 + ((n) >> 1)) * 65536, ut); } while (0)
  Loader<3, 4> l3; Loader<4, 4> l4;
  if constexpr (LD) { l3.init(c); l4.init(c); }
  bf16x8 ones;
  {
    const __bf16 v = (((lane >> 2) ^ (lane >> 4)) & 1) ? (__bf16)0.0f : (__bf16)1.0f;
#pragma unroll
    for (int j = 0; j < 8; ++j) ones[j] = v;
  }
#define RDK(dst, P, n) dst = *(const LAS bf16x8*)((P) + (n) * 1024)
#define RDV(dst, P, n) dst = *(const LAS bf16x8*)((P) + 8192 + (n) * 1024)
  bf16x8 k0a, k0b, k1a, k1b, v0a, v0b, v1a, v1b;
  f32x16 sA, sB, nm0, nm1, o0 = {}, o1 = {};
  f32x4 l0 = {}, l1 = {};
  u32x4 pA0, pA1, pB0 = {}, pB1 = {};
  lptr kb = smem + grp * 16384 + lane * 16;
  int cur = (S_AT & 3) * SLOT;
  RDK(k0a, kb + cur, 0); RDK(k0b, kb + cur, 1); RDK(k1a, kb + cur, 2); RDK(k1b, kb + cur, 3);
  RDV(v0a, kb + cur, 0); RDV(v0b, kb + cur, 1);
  {
    const f32x16 z = {};
    f32x16 s0 = MFMA32(k0a, qa0, z); s0 = MFMA32(k0b, qb0, s0);
    f32x16 s1 = MFMA32(k1a, qa0, z); s1 = MFMA32(k1b, qb0, s1);
    nm0 = splat16(-(__builtin_ceilf(halfmax(__builtin_fmaxf(max16(s0), max16(s1)))) + REF_BIAS));
    s0 = MFMA32(k0a, qa1, z); s0 = MFMA32(k0b, qb1, s0);
    s1 = MFMA32(k1a, qa1, z); s1 = MFMA32(k1b, qb1, s1);
    nm1 = splat16(-(__builtin_ceilf(halfmax(__builtin_fmaxf(max16(s0), max16(s1)))) + REF_BIAS));
  }
  PIN(nm0); PIN(nm1);
  sA = MFMA32(k0a, qa0, nm0); sA = MFMA32(k0b, qb0, sA);
  v1a = v0a; v1b = v0a;
  STAMP(4);
  int t = 0;
  float tl0 = 0.f, tl1 = 0.f, tl2 = 0.f, tl3 = 0.f;
#define NOP (void)0
#define UE(KA, KB_, VA, VB, R0, R1, X2, X3) UNIT(sA, pA0, pA1, sB, KA, KB_, qa1, qb1, nm1, o1, l1, VA, VB, pB0, pB1, R0, R1, X2, X3)
#define UO(KA, KB_, VA, VB, R0, R1, X2, X3) UNIT(sB, pB0, pB1, sA, KA, KB_, qa0, qb0, nm0, o0, l0, VA, VB, pA0, pA1, R0, R1, X2, X3)
#pragma unroll 1
  while (t < 8) {
    bool trig;
#pragma unroll 1
    do {
      lptr cp = kb + cur;
      cur = cur == 3 * SLOT ? 0 : cur + SLOT;
      lptr np = kb + cur;
      SBAR();
      UE(k0a, k0b, v1a, v1b, RDK(k1a, cp, 2), RDK(k1b, cp, 3), URD(0), UST(0));
      UO(k1a, k1b, v0a, v0b, RDV(v1a, cp, 2), RDV(v1b, cp, 3), URD(1), UST(1));
      UE(k1a, k1b, v0a, v0b, RDK(k0a, cp, 4), RDK(k0b, cp, 5), URD(2), UST(2));
      UO(k0a, k0b, v1a, v1b, RDV(v0a, cp, 4), RDV(v0b, cp, 5), URD(3), UST(3));
      UE(k0a, k0b, v1a, v1b, RDK(k1a, cp, 6), RDK(k1b, cp, 7), NOP, NOP);
      UO(k1a, k1b, v0a, v0b, RDV(v1a, cp, 6), RDV(v1b, cp, 7), NOP, NOP);
      if constexpr (LD) {
        drain();
        if (t == 5 && wq == 2) await(uflags, 16, tag, lane, smem);
        bar_l();
        if (t < 4) l3.stage(c, t + 4, S_AT + t + 4);
        else if (t > 4) l4.stage(c, t - 5, S_P4 + t - 5);
        else if (wq == 2) mail(uflags, 16, lane, c.lds0);
        if (t == 2) set_flag(myflag, tag);
      } else {
        bar_vl<4>();
      }
      SBAR();
      UE(k1a, k1b, v0a, v0b, RDK(k0a, np, 0), RDK(k0b, np, 1), NOP, NOP);
      UO(k0a, k0b, v1a, v1b, RDV(v0a, np, 0), RDV(v0b, np, 1); trig = __any(l0[0] + l1[0] > L_LIMIT), NOP, NOP);
      ++t;
    } while (t < 8 && !trig);
    if (t >= 8) break;
    asm volatile("s_nop 7" : "+v"(l0), "+v"(l1));
    pB1[2] = pk(tl0, tl1); pB1[3] = pk(tl2, tl3);
    o1 = MFMA32(v1a, BC(pB0), o1); o1 = MFMA32(v1b, BC(pB1), o1);
    l1 = MFMA16(ones, BC(pB0), l1); l1 = MFMA16(ones, BC(pB1), l1);
    pB0 = u32x4{}; pB1 = u32x4{}; tl0 = 0.f; tl1 = 0.f; tl2 = 0.f; tl3 = 0.f;
    float f0, f1;
    RECENTRE(nm0, o0, l0, f0);
    RECENTRE(nm1, o1, l1, f1);
#pragma unroll
    for (int r = 0; r < 16; ++r) sA[r] -= f0;
    asm volatile("s_nop 1" : "+v"(l0), "+v"(l1), "+v"(pB0), "+v"(pB1));
  }
  STAMP(5);
  asm volatile("s_nop 7" : "+v"(l0), "+v"(l1));
  pB1[2] = pk(tl0, tl1); pB1[3] = pk(tl2, tl3);
  o1 = MFMA32(v1a, BC(pB0), o1); o1 = MFMA32(v1b, BC(pB1), o1);
  l1 = MFMA16(ones, BC(pB0), l1); l1 = MFMA16(ones, BC(pB1), l1);
  lptr mg = smem + (S_MG & 3) * SLOT + lane * 16;
  {
    const f32x16& og = LD ? o0 : o1;
    lptr d = mg + ((1 - grp) * 4 + wq) * 5120;
#pragma unroll
    for (int q = 0; q < 4; ++q) *(LAS f32x4*)(d + q * 1024) = f32x4{og[4 * q], og[4 * q + 1], og[4 * q + 2], og[4 * q + 3]};
    *(LAS f32x4*)(d + 4096) = f32x4{LD ? l0[0] : l1[0], LD ? nm0[0] : nm1[0], 0.f, 0.f};
  }
  bar_l();
  f32x16 o = LD ? o1 : o0;
  float l = LD ? l1[0] : l0[0];
  {
    lptr d = mg + (grp * 4 + wq) * 5120;
    const f32x4 e = *(const LAS f32x4*)(d + 4096);
    const float nmk = LD ? nm1[0] : nm0[0], nmin = __builtin_fminf(nmk, e[1]);
    const int ek = (int)__builtin_fmaxf(nmin - nmk, -400.f), eo = (int)__builtin_fmaxf(nmin - e[1], -400.f);
    l = __builtin_amdgcn_ldexpf(l, ek) + __builtin_amdgcn_ldexpf(e[0], eo);
#pragma unroll
    for (int q = 0; q < 4; ++q) {
      const f32x4 x = *(const LAS f32x4*)(d + q * 1024);
#pragma unroll
      for (int r = 0; r < 4; ++r) o[4 * q + r] = __builtin_amdgcn_ldexpf(o[4 * q + r], ek) + __builtin_amdgcn_ldexpf(x[r], eo);
    }
  }
  float chk = l;
#pragma unroll
  for (int r = 0; r < 16; ++r) chk += __builtin_fabsf(o[r]);
  u32x4 x, y;
  if (__builtin_expect(__any(!(chk < 3.0e38f) || !(l > 0.f)), 0)) attn_safe(Kh, Vh, Qw + grp * 2048, lane, x, y);
  else pack16(o, 1.0f / l, x, y);
  const unsigned vo = ooff + grp * 65536 + ((lane >> 4) & 1) * 32768 + (lane >> 5) * 256 + (lane & 15) * 16;
  st_wt(rO, vo, x); st_wt(rO, vo + 512, y);
}

__device__ __forceinline__ void store_out(const f32x4 (&acc)[4][2], u16* out, int row0, int col0, int lane) {
#pragma unroll
  for (int mi = 0; mi < 4; ++mi)
    *(u32x4*)(out + (row0 + mi * 16 + (lane & 15)) * 2048 + col0 + (lane >> 4) * 8) = natural(pack2(acc[mi][0], acc[mi][1], 1.0f));
}

__global__ __launch_bounds__(512, 2) void fused(const u16* __restrict__ xA, const u16* __restrict__ Wsr, const u16* __restrict__ Wsi,
                                                const u16* __restrict__ Wdr, const u16* __restrict__ Wdi, const u16* __restrict__ xB,
                                                const u16* __restrict__ wqkv, const u16* __restrict__ wo, u16* __restrict__ out,
                                                char* ws) {
  extern __shared__ __attribute__((aligned(16))) char smem_[];
  lptr smem = (lptr)smem_;
  const int lane = threadIdx.x & 63, w = __builtin_amdgcn_readfirstlane(threadIdx.x >> 6), wm = (w >> 1) & 1, wn = w & 1;
  const int xcd = blockIdx.x & 7, i = blockIdx.x >> 3;
  gu32* flags = (gu32*)ws + 64;
  char* QKV = ws + (1 << 20);
  char* Of = QKV + (12 << 20);
  char* Uf = Of + (4 << 20);
  const int j2 = xcd * 2 + (i >> 4), tb1 = i & 15;
  const int tbA = xcd * 2 + (i >> 4), im = (i >> 3) & 1, nb2 = i & 7, eb = i & 15;
  const int head = xcd * 4 + (i >> 3), qb = i & 7;
  const int tb5 = (xcd >> 1) * 4 + (i & 3), nb5 = (xcd & 1) * 8 + (i >> 2);
  const char* Kh = QKV + (4 << 20) + head * 131072;
  const char* Vh = QKV + (8 << 20) + head * 131072;
  const int qt = qb * 8 + (w & 3) * 2;
  const char* Qw = QKV + (head * 64 + qt) * 2048;
  const unsigned ooff = qt * 65536 + head * 1024;
  Ctx c;
  c.lane = lane; c.lw = w & 3; c.lds0 = (unsigned)(unsigned long)smem;
  STAMP(0);
  if (w >= 4) {
    c.a1 = (const char*)(xB + tb1 * 128 * D_); c.b1 = (const char*)(wqkv + j2 * 64);
    c.a2 = (const char*)((im ? Wsi : Wsr) + tbA * 128 * S_); c.b2 = (const char*)(xA + nb2 * 128);
    c.kv = Kh;
    c.a4 = Uf + tbA * 8 * 65536; c.b4r = (const char*)(Wdr + eb * 64 * D_); c.b4i = (const char*)(Wdi + eb * 64 * D_);
    c.a5 = Of + tb5 * 8 * 32768; c.b5 = (const char*)(wo + nb5 * 64);
    gu32 *fq = flags + (head >> 1) * 16, *fo = flags + 512 + (tb5 >> 1) * 32;
    unsigned tag = 0;
    bf16x8 q[4];
    auto load_q = [&]() {
#pragma unroll
      for (int k = 0; k < 4; ++k) blds<1>(mkrs(Qw), lane * 16, k * 1024, c.lds0 + c.lw * SLOT + 32768 + k * 1024);
    };
    auto read_q = [&]() {
#pragma unroll
      for (int k = 0; k < 4; ++k) q[k] = *(const LAS bf16x8*)(smem + c.lw * SLOT + 32768 + k * 1024 + lane * 16);
    };
    if (w < 6) {
      {
        Loader<1, 2> l1; l1.init(c);
#pragma unroll 1
        for (int s = 0; s < 3; ++s) l1.stage(c, s, s);
#pragma unroll 1
        for (int s = -1; s < 12; ++s) { bar_v<40>(); l1.stage(c, s + 4, s + 4); }
      }
      {
        Loader<2, 2> l2; l2.init(c);
#pragma unroll 1
        for (int s = 12; s < 44; ++s) { bar_v<32>(); l2.stage(c, s - 12, s + 4); }
      }
      Loader<3, 2> l3; l3.init(c);
      bar_v<32>();
      load_q();
      l3.stage(c, 0, 48);
#pragma unroll 1
      for (int s = 45; s < 47; ++s) { bar_v<32>(); l3.stage(c, s - 44, s + 4); }
      asm volatile("s_waitcnt vmcnt(32)" ::: "memory");
      read_q();
      bar_l();
      l3.stage(c, 3, 51);
    } else {
      const bool u = w == 7;
      const int b = blockIdx.x;
      if (u) {
        const unsigned vo = lane < 48 ? (tb1 * 16 + (lane & 15)) * 6144 + (lane >> 4) * 2048 : 0u;
#pragma unroll
        for (int k = PF0; k < 4; ++k) touch(c.b1, vo, k * 256 * 6144);
      } else {
        const unsigned vo = ((lane >> 2) & 7) * 2048 + (lane & 3) * 128;
#pragma unroll
        for (int k = PF0; k < 4; ++k) touch(xB + b * 8 * D_, vo, k * 512);
      }
      asm volatile("s_load_dword %0, %1, 0x0\n\ts_waitcnt lgkmcnt(0)" : "=s"(tag) : "s"(ws) : "memory");
      tag = tag * 1664525u + 1013904223u;
      const void* p2 = u ? (const void*)(xA + b * D_) : (const void*)(c.a2 + nb2 * 16 * 4096);
      const unsigned vo2 = u ? (lane & 15) * 128 : (lane >> 2) * 4096 + (lane & 3) * 128;
      const char* pKVo = QKV + (4 << 20) + ((j2 * 2) * 64 + tb1 * 4 + (w & 1) * 2) * 2048;
      bar_l();
#pragma unroll 1
      for (int s = 0; s < 44; ++s) {
        if (s == 27) drain();
        bar_l();
        if (u) { if (s >= PFS && s < PFS + 8 * PFI && (s - PFS) % PFI == 0) touch(p2, vo2, ((s - PFS) / PFI) * 256 * 2048); }
        else if (s >= PWS && s < PWS + 8 * PWI && (s - PWS) % PWI == 0) touch(p2, vo2, ((s - PWS) / PWI) * 512);
        if (s >= 16 && s < 24) {
          const int n = s - 16;
#pragma unroll
          for (int h = 0; h < 2; ++h) {
            const u32x4 v = *(const LAS u32x4*)(smem + ((w & 1) * 2 + h) * SLOT + 32768 + n * 1024 + lane * 16);
            __builtin_amdgcn_raw_buffer_store_b128(v, mkrs(pKVo), lane * 16, h * 131072 + (n >> 2) * (4 << 20) + (n & 3) * 1024, 16);
          }
        }
        if (s == 27) set_flag(flags + j2 * 16 + tb1, tag);
        if (s == 40 && !u) mail(fq, 16, lane, c.lds0);
      }
      drain();
      if (!u) await(fq, 16, tag, lane, smem);
      bar_l();
      load_q();
      bar_l(); bar_l();
      drain();
      read_q();
      bar_l();
      touch(u ? Wdi : Wdr, lane * 128, b * 8192);
      touch(wo, (lane & 31) * 128, b * 8192 + (u ? 4096 : 0));
    }
    STAMP(3);
    attention<true>(c, q, Qw, Kh, Vh, mkrs(Of), ooff, 1, w & 3, nullptr, flags + 1024 + tbA * 16, flags + 1024 + tbA * 16 + (i & 15), tag, smem);
    STAMP(6);
    bar_vl<2>();
    if (w < 6) {
      {
        Loader<4, 2> l4; l4.init(c);
        l4.stage(c, 3, S_P4 + 3);
#pragma unroll 1
        for (int s = S_P4; s < S_P4 + 2; ++s) { bar_v<26>(); l4.stage(c, s + 4 - S_P4, s + 4); }
#pragma unroll 1
        for (int s = S_P4 + 2; s < S_P5 - 4; ++s) { bar_v<24>(); l4.stage(c, s + 4 - S_P4, s + 4); }
      }
      Loader<5, 2> l5; l5.init(c);
#pragma unroll 1
      for (int s = S_P5 - 4; s < S_END - 4; ++s) { bar_v<24>(); l5.stage(c, s + 4 - S_P5, s + 4); }
      bar_v<24>(); bar_v<12>(); bar_v<0>(); bar_v<0>();
    } else {
#pragma unroll 1
      for (int s = S_P4; s < S_P5 - 4; ++s) {
        if (s == S_P4 + 4) drain();
        bar_l();
        if (s == S_P4 + 4) set_flag(flags + 512 + qb * 32 + head, tag);
        if (s == S_P5 - 8 && w == 6) mail(fo, 32, lane, c.lds0);
        if (blockIdx.x == 0 && w == 6) {
          if (s == S_P4 + 8) blds<1>(mkrs((const void*)flags), lane * 16, 0, c.lds0 + MAIL + 1024);
          if (s == S_P4 + 16) {
            drain();
            u32x4 f = *(volatile LAS u32x4*)(smem + MAIL + 1024 + lane * 16);
#pragma unroll 1
            for (unsigned spins = 0; spins < 400000u && !__all(f[0] == tag && f[1] == tag && f[2] == tag && f[3] == tag); ++spins) {
              __builtin_amdgcn_s_sleep(2);
              f = __builtin_amdgcn_raw_buffer_load_b128(mkrs((const void*)flags), lane * 16, 0, 16);
            }
            set_flag((gu32*)ws, tag);
          }
        }
      }
      drain();
      if (w == 6) await(fo, 32, tag, lane, smem);
      bar_l();
    }
    return;
  }
  const unsigned lds0 = c.lds0;
  lptr park = smem + w * SLOT + 32768;
  bar_l();
  {
    FragRd<OP_ROW, 1> ra; ra.init(wm * 4, lane, lds0 + (S_P1 & 3) * SLOT);
    FragRd<OP_TR, 2> rb; rb.init(wn * 2, lane, lds0 + (S_P1 & 3) * SLOT + 16384);
    f32x4 acc[4][6] = {};
    consume<4, 6, 4, S_P1, 16, 0>(acc, ra, rb);
    STAMP(1);
    const rsrc_t rQ = mkrs(QKV + ((j2 * 2 + wn) * 64 + tb1 * 4 + wm * 2) * 2048);
    const int cc = lane & 15, g = lane >> 4;
#pragma unroll
    for (int mi = 0; mi < 4; ++mi) {
      const unsigned blk = (mi >> 1) * 2048 + g * 512;
      st_wt(rQ, blk + ((mi & 1) * 16 + cc) * 16, pack2(acc[mi][0], acc[mi][1], 0.17677669529663687f * 1.4426950408889634f));
      *(LAS u32x4*)(park + blk + ((cc >> 3) * 16 + (mi & 1) * 8 + (cc & 7)) * 16) = pack2(acc[mi][2], acc[mi][3], 1.0f);
      *(LAS u32x4*)(park + 4096 + blk + ((mi & 1) * 16 + cc) * 16) = pack2(acc[mi & 2][4 + (mi & 1)], acc[(mi & 2) + 1][4 + (mi & 1)], 1.0f);
    }
    STAMP(2);
  }
  bf16x8 q[4];
  {
    FragRd<OP_ROW, 1> ra; ra.init(wm * 4, lane, lds0 + (S_P2 & 3) * SLOT);
    FragRd<OP_TR, 4> rb; rb.init(0, lane, lds0 + (S_P2 & 3) * SLOT + 16384 + wn * 8192);
    f32x4 acc[4][4] = {};
    consume<4, 4, 4, S_P2, 32, 11>(acc, ra, rb);
    STAMP(3);
#pragma unroll
    for (int k = 0; k < 4; ++k) q[k] = *(const LAS bf16x8*)(park + k * 1024 + lane * 16);
    const float sc = im ? -1.0f : 1.0f;
#pragma unroll
    for (int mi = 0; mi < 4; ++mi)
#pragma unroll
      for (int n2 = 0; n2 < 2; ++n2)
        *(LAS u32x4*)(park + (mi * 2 + n2) * 1024 + lane * 16) = natural(pack2(acc[mi][2 * n2], acc[mi][2 * n2 + 1], sc));
  }
  attention<false>(c, q, Qw, Kh, Vh, mkrs(Of), ooff, 0, w, Uf + (tbA * 8 + wm * 4) * 65536 + (im * 32 + nb2 * 4 + wn * 2) * 1024,
                   nullptr, nullptr, 0u, smem);
  STAMP(6);
  bar_l();
  {
    FragRd<OP_FRAG, 1> ra; ra.init(wm * 4, lane, lds0 + (S_P4 & 3) * SLOT);
    FragRd<OP_ROW, 1> rb; rb.init(wn * 2, lane, lds0 + (S_P4 & 3) * SLOT + 16384);
    f32x4 acc[4][2] = {};
    consume<4, 2, 2, S_P4, 32, 4>(acc, ra, rb);
    STAMP(7);
    store_out(acc, out, tbA * 128 + wm * 64, eb * 64 + wn * 32, lane);
  }
  {
    FragRd<OP_FRAG, 1> ra; ra.init(wm * 4, lane, lds0 + (S_P5 & 3) * SLOT);
    FragRd<OP_TRP, 2> rb; rb.init(wn * 2, lane, lds0 + (S_P5 & 3) * SLOT + 16384);
    f32x4 acc[4][2] = {};
    consume<4, 2, 2, S_P5, 16, 0>(acc, ra, rb);
    STAMP(8);
    store_out(acc, out, tb5 * 128 + wm * 64, 1024 + nb5 * 64 + wn * 32, lane);
  }
}

extern "C" void kernel_launch(void* const* d_in, const int* in_sizes, int n_in, void* d_out, int out_size, void* d_ws,
                              size_t ws_size, hipStream_t stream) {
  (void)hipFuncSetAttribute((const void*)fused, hipFuncAttributeMaxDynamicSharedMemorySize, LDS_BYTES);
  fused<<<256, 512, LDS_BYTES, stream>>>((const u16*)d_in[0], (const u16*)d_in[1], (const u16*)d_in[2], (const u16*)d_in[3],
                                         (const u16*)d_in[4], (const u16*)d_in[5], (const u16*)d_in[6], (const u16*)d_in[7],
                                         (u16*)d_out, (char*)d_ws);
}
```

```cpp
#include <hip/hip_runtime.h>
#include <hip/hip_bf16.h>
#include <type_traits>

typedef unsigned short u16;
typedef __attribute__((ext_vector_type(8))) __bf16 bf16x8;
typedef __attribute__((ext_vector_type(2))) __bf16 bf16x2;
typedef __attribute__((ext_vector_type(2))) float f32x2;
typedef __attribute__((ext_vector_type(4))) float f32x4;
typedef __attribute__((ext_vector_type(16))) float f32x16;
typedef __attribute__((ext_vector_type(4))) unsigned u32x4;
typedef __attribute__((ext_vector_type(2))) unsigned u32x2;
typedef __attribute__((ext_vector_type(4))) short s16x4;
#define LAS __attribute__((address_space(3)))
#define GAS __attribute__((address_space(1)))
typedef LAS char* lptr;
typedef GAS unsigned gu32;
typedef __amdgpu_buffer_rsrc_t rsrc_t;

#define SBAR() __builtin_amdgcn_sched_barrier(0)
#define PIN(x) asm volatile("" : "+v"(x))
#define MFMA32(a, b, c) __builtin_amdgcn_mfma_f32_32x32x16_bf16((a), (b), (c), 0, 0, 0)
#define MFMA16(a, b, c) __builtin_amdgcn_mfma_f32_16x16x32_bf16((a), (b), (c), 0, 0, 0)
#define BC(x) __builtin_bit_cast(bf16x8, x)
#define RLX_AGENT __ATOMIC_RELAXED, __HIP_MEMORY_SCOPE_AGENT
template <int N> __device__ __forceinline__ void bar_vl() { asm volatile("s_waitcnt vmcnt(%0) lgkmcnt(0)\n\ts_barrier" ::"n"(N) : "memory"); }

#define STAMP(I)
#define ST2(W, I)
#define ST3(T, I)

#define APOL 1
#define CUP 0
#define CODEPF 0
#define NQE 2
#define LK 1
#define QS 2
#define QI 0
#define CS 2
#define DNT 0
#define PWS 4
#define PWI 3
#define PFS 2
#define PFI 3
constexpr int S_ = 2048, D_ = 1024, SLOT = 40960, LDS_BYTES = 4 * SLOT;
enum { OP_ROW = 0, OP_FRAG = 1, OP_TR = 2, OP_TRP = 3 };

__device__ __forceinline__ unsigned pk(float lo, float hi) {
  f32x2 v = {lo, hi};
  return __builtin_bit_cast(unsigned, __builtin_convertvector(v, bf16x2));
}
__device__ __forceinline__ rsrc_t mkrs(const void* p, bool on = true) {
  return __builtin_amdgcn_make_buffer_rsrc((void*)p, 0, on ? 0x7fffffff : 0, 0x00020000);
}
template <int POL = 0>
__device__ __forceinline__ void blds(rsrc_t r, unsigned voff, unsigned soff, unsigned l) {
  if constexpr (POL == 1) asm volatile("s_mov_b32 m0, %0\n\ts_nop 0\n\tbuffer_load_dwordx4 %1, %2, %3 offen sc1 lds" ::"s"(l), "v"(voff), "s"(r), "s"(soff) : "memory");
  else if constexpr (POL == 2) asm volatile("s_mov_b32 m0, %0\n\ts_nop 0\n\tbuffer_load_dwordx4 %1, %2, %3 offen nt lds" ::"s"(l), "v"(voff), "s"(r), "s"(soff) : "memory");
  else asm volatile("s_mov_b32 m0, %0\n\ts_nop 0\n\tbuffer_load_dwordx4 %1, %2, %3 offen lds" ::"s"(l), "v"(voff), "s"(r), "s"(soff) : "memory");
}
__device__ __forceinline__ bf16x8 ld_dev(rsrc_t r, unsigned voff, unsigned soff = 0) {
  return __builtin_bit_cast(bf16x8, __builtin_amdgcn_raw_buffer_load_b128(r, voff, soff, 16));
}
__device__ __forceinline__ void st_wt(rsrc_t r, unsigned voff, u32x4 v) { __builtin_amdgcn_raw_buffer_store_b128(v, r, voff, 0, 16); }

template <int KIND>
__device__ __forceinline__ unsigned piece_off(int p, int i, int ld) {
  if constexpr (KIND == OP_ROW) return (8 * p + (i >> 3)) * ld + (((i & 7) ^ (((p & 1) << 2) | (i >> 4))) << 4);
  else if constexpr (KIND == OP_FRAG) return (p >> 1) * ld + (p & 1) * 1024 + i * 16;
  else if constexpr (KIND == OP_TR) return (8 * p + (i >> 3)) * ld + (((i & 7) ^ ((((i >> 4) & 1) | ((p & 1) << 1)) << 1)) << 4);
  else return (8 * p + (i >> 3)) * ld + (((i & 7) ^ ((i >> 4) << 1)) << 4);
}
template <int KIND, int NBASE>
struct FragRd {
  static constexpr int NB = KIND == OP_ROW ? 2 : (KIND == OP_FRAG ? 1 : NBASE);
  static constexpr int OPS = KIND >= OP_TR ? 2 : 1;
  unsigned base[NB];
  __device__ __forceinline__ void init(int b0, int lane, unsigned img) {
    const int r = lane & 15, g = lane >> 4;
    if constexpr (KIND == OP_ROW) {
#pragma unroll
      for (int ks = 0; ks < 2; ++ks) base[ks] = img + (b0 * 16 + r) * 128 + (((4 * ks + g) ^ ((r >> 1) & 7)) << 4);
    } else if constexpr (KIND == OP_FRAG) {
      base[0] = img + b0 * 2048 + lane * 16;
    } else {
      const int q = r >> 2, p = lane & 3, f = ((q >> 1) & 1) | ((g & 1) << 1);
      const int row = (KIND == OP_TR ? 8 * g : 16 * (g >> 1) + 4 * (g & 1)) + q;
#pragma unroll
      for (int b = 0; b < NBASE; ++b) base[b] = img + row * 128 + (((((b0 + b) ^ f) << 1) | (p >> 1)) << 4) + (p & 1) * 8;
    }
  }
  __device__ __forceinline__ void step(int k, int d) { base[k] += d; PIN(base[k]); }
  __device__ __forceinline__ void rd(u32x4& f, int b, int ks, int half) const {
    if constexpr (KIND == OP_ROW) f = *(const LAS u32x4*)(unsigned long)(base[ks] + b * 2048);
    else if constexpr (KIND == OP_FRAG) f = *(const LAS u32x4*)(unsigned long)(base[0] + b * 2048 + ks * 1024);
    else {
      const u32x2 x = __builtin_bit_cast(u32x2, __builtin_amdgcn_ds_read_tr16_b64_v4i16(
          (LAS s16x4*)(unsigned long)(base[b % NBASE] + (b / NBASE) * 8192 + ks * 4096 + half * (KIND == OP_TR ? 512 : 1024))));
      f[2 * half] = x[0]; f[2 * half + 1] = x[1];
    }
  }
};

__device__ __forceinline__ u32x4 pack2(const f32x4& x, const f32x4& y, float sc) {
  return u32x4{pk(x[0] * sc, x[1] * sc), pk(x[2] * sc, x[3] * sc), pk(y[0] * sc, y[1] * sc), pk(y[2] * sc, y[3] * sc)};
}
__device__ __forceinline__ u32x4 natural(u32x4 c) {
  auto x = __builtin_amdgcn_permlane32_swap(c[0], c[2], false, false);
  auto y = __builtin_amdgcn_permlane32_swap(c[1], c[3], false, false);
  auto p = __builtin_amdgcn_permlane16_swap(x[0], x[1], false, false);
  auto q = __builtin_amdgcn_permlane16_swap(y[0], y[1], false, false);
  return u32x4{p[0], q[0], p[1], q[1]};
}

__device__ __forceinline__ void bar_l() { asm volatile("s_waitcnt lgkmcnt(0)\n\ts_barrier" ::: "memory"); }
template <int N> __device__ __forceinline__ void bar_v() { asm volatile("s_waitcnt vmcnt(%0)\n\ts_barrier" ::"n"(N) : "memory"); }
__device__ __forceinline__ void drain() { asm volatile("s_waitcnt vmcnt(0)" ::: "memory"); }
__device__ __forceinline__ void set_flag(gu32* flag, unsigned tag) {
  if (threadIdx.x == 384) __hip_atomic_store(flag, tag, RLX_AGENT);
}
constexpr int MAIL = 32768 + 4096;
__device__ __forceinline__ void mail(gu32* flags, int n, int lane, unsigned lds0) {
  asm volatile("s_mov_b32 m0, %0\n\ts_nop 0\n\tbuffer_load_dword %1, %2, 0 offen sc1 lds"
               ::"s"(lds0 + MAIL), "v"((lane < n ? lane : 0) * 4), "s"(mkrs((const void*)flags)) : "memory");
}
__device__ __forceinline__ void await(gu32* flags, int n, unsigned tag, int lane, lptr smem) {
  if (__builtin_expect(!__all(*(volatile LAS unsigned*)(smem + MAIL + lane * 4) == tag), 0)) {
#pragma unroll 1
    for (unsigned spins = 0; spins < 400000u; ++spins) {
      if (__all(__hip_atomic_load(flags + (lane < n ? lane : 0), RLX_AGENT) == tag)) break;
      __builtin_amdgcn_s_sleep(2);
    }
  }
  __builtin_amdgcn_fence(__ATOMIC_ACQUIRE, "wavefront");
}
#define WAIT_LGKM(n) __builtin_amdgcn_s_waitcnt(0xC07F | ((n) << 8))

template <int MI, int NI, int NSWAP, int S0, int NT, int ND, class RA, class RB>
__device__ __forceinline__ void consume(f32x4 (&acc)[MI][NI], RA& ra, RB& rb) {
  u32x4 a0[MI], b0[NI], a1[MI], b1[NI];
  constexpr int NM = MI * NI, NBO = NI * RB::OPS, NR = MI + NBO, NU = RA::NB + RB::NB;
  auto rdop = [&](u32x4(&na)[MI], u32x4(&nb)[NI], int ks, int z) {
    if (z == 0) ra.rd(na[0], 0, ks, 0);
    else if (z <= NBO) rb.rd(nb[(z - 1) / RB::OPS], (z - 1) / RB::OPS, ks, (z - 1) % RB::OPS);
    else ra.rd(na[z - NBO], z - NBO, ks, 0);
  };
  auto mma = [&](const u32x4(&fa)[MI], const u32x4(&fb)[NI], int i) {
    const int mi = i / NI, ni = i % NI;
    acc[mi][ni] = ni < NSWAP ? MFMA16(BC(fb[ni]), BC(fa[mi]), acc[mi][ni]) : MFMA16(BC(fa[mi]), BC(fb[ni]), acc[mi][ni]);
    SBAR();
  };
#pragma unroll
  for (int z = 0; z < NR; ++z) { rdop(a0, b0, 0, z); SBAR(); }
  int slot = S0 & 3;
#pragma unroll 1
  for (int seg = ND ? 0 : 1; seg < 2; ++seg) {
#pragma unroll 1
    for (int t = seg ? NT - ND : ND; t > 0; --t) {
      slot = (slot + 1) & 3;
      const int d = slot == 0 ? -3 * SLOT : SLOT;
      WAIT_LGKM(MI - 1);
      SBAR();
#pragma unroll
      for (int i = 0; i < NM + 2; ++i) {
        if (i < NM) mma(a0, b0, i);
        else { if (i == NM) { WAIT_LGKM(0); SBAR(); } mma(a1, b1, i - NM); }
        if (i < NR) rdop(a1, b1, 1, i);
        else {
          constexpr int G = NM + 2 - NR;
#pragma unroll
          for (int u = (i - NR) * NU / G; u < (i - NR + 1) * NU / G; ++u) { if (u < RA::NB) ra.step(u, d); else rb.step(u - RA::NB, d); }
        }
        SBAR();
      }
      ST2(0, S0 + (seg ? NT - t : ND - t));
      asm volatile("s_barrier" ::: "memory");
      ST2(1, S0 + (seg ? NT - t : ND - t));
      SBAR();
      constexpr int G1 = NM - 2;
#pragma unroll
      for (int i = 0; i < G1; ++i) {
        if (NR > G1 && i < NR - G1) { rdop(a0, b0, 0, 2 * i); SBAR(); }
        mma(a1, b1, i + 2);
        const int z = NR > G1 ? (i < NR - G1 ? 2 * i + 1 : i + NR - G1) : i;
        if (z < NR) rdop(a0, b0, 0, z);
        SBAR();
      }
    }
    if (ND) drain();
  }
}

__device__ __forceinline__ void pack16(const f32x16& a, float sc, u32x4& c0, u32x4& c1) {
  c0 = u32x4{pk(a[0] * sc, a[1] * sc), pk(a[2] * sc, a[3] * sc), pk(a[4] * sc, a[5] * sc), pk(a[6] * sc, a[7] * sc)};
  c1 = u32x4{pk(a[8] * sc, a[9] * sc), pk(a[10] * sc, a[11] * sc), pk(a[12] * sc, a[13] * sc), pk(a[14] * sc, a[15] * sc)};
}
__device__ __forceinline__ float halfmax(float m) {
  auto rr = __builtin_amdgcn_permlane32_swap(__float_as_uint(m), __float_as_uint(m), false, false);
  return __builtin_fmaxf(__uint_as_float(rr[0]), __uint_as_float(rr[1]));
}
__device__ __forceinline__ float max16(const f32x16& s) {
  float a = __builtin_fmaxf(s[0], s[1]);
#pragma unroll
  for (int r = 2; r < 16; ++r) a = __builtin_fmaxf(a, s[r]);
  return a;
}
__device__ __forceinline__ f32x16 splat16(float v) {
  f32x16 r;
#pragma unroll
  for (int i = 0; i < 16; ++i) r[i] = v;
  return r;
}
#define RBIAS 96.0f
constexpr float REF_BIAS = RBIAS;
constexpr float L_LIMIT = 1.099511627776e12f;

__device__ __forceinline__ void attn_safe(const char* Kh, const char* Vh, const char* Qt, int lane, u32x4& c0, u32x4& c1) {
  const rsrc_t rq = mkrs(Qt), rk = mkrs(Kh), rv = mkrs(Vh);
  const bf16x8 q0 = ld_dev(rq, lane * 16), q1 = ld_dev(rq, 1024 + lane * 16);
  float m = -3.0e38f, l = 0.f;
  f32x16 o = {};
  const f32x16 z = {};
#pragma unroll 1
  for (int kt = 0; kt < 64; ++kt) {
    const bf16x8 k0 = ld_dev(rk, lane * 16, kt * 2048), k1 = ld_dev(rk, 1024 + lane * 16, kt * 2048);
    const bf16x8 v0 = ld_dev(rv, lane * 16, kt * 2048), v1 = ld_dev(rv, 1024 + lane * 16, kt * 2048);
    f32x16 s = MFMA32(k0, q0, z);
    s = MFMA32(k1, q1, s);
    const float mn = __builtin_fmaxf(m, halfmax(max16(s)));
    const float al = __builtin_amdgcn_exp2f(m - mn);
    m = mn;
    float rs = 0.f;
#pragma unroll
    for (int r = 0; r < 16; ++r) { s[r] = __builtin_amdgcn_exp2f(s[r] - mn); o[r] *= al; }
    u32x4 p0, p1;
    pack16(s, 1.0f, p0, p1);
#pragma unroll
    for (int r = 0; r < 4; ++r) {
      rs += __uint_as_float(p0[r] << 16) + __uint_as_float(p0[r] & 0xffff0000u);
      rs += __uint_as_float(p1[r] << 16) + __uint_as_float(p1[r] & 0xffff0000u);
    }
    l = l * al + rs;
    o = MFMA32(v0, BC(p0), o);
    o = MFMA32(v1, BC(p1), o);
  }
  auto rr = __builtin_amdgcn_permlane32_swap(__float_as_uint(l), __float_as_uint(l), false, false);
  l = __uint_as_float(rr[0]) + __uint_as_float(rr[1]);
  pack16(o, 1.0f / l, c0, c1);
}

#define EXP2(x) __builtin_amdgcn_exp2f(x)
#define X1(S, i) do { S[i] = EXP2(S[i]); PIN(S[i]); SBAR(); } while (0)
#define XT(T, S, i) do { T = EXP2(S[i]); PIN(T); SBAR(); } while (0)
#define CV(P, j, S, i) do { P[j] = pk(S[i], S[i + 1]); PIN(P[j]); SBAR(); } while (0)
#define CT(P, j, A, B) do { P[j] = pk(A, B); PIN(P[j]); SBAR(); } while (0)
#define SUMP(L, P) asm volatile("v_mfma_f32_16x16x32_bf16 %0, %1, %2, %0" : "+v"(L) : "v"(ones), "v"(P))
#define UNIT(S, Pn0, Pn1, Sn, KA, KB_, Qa, Qb, NM, O, L, VA, VB, Pp0, Pp1, RD0, RD1, X2, X3)     \
  do {                                                                                          \
    Sn = MFMA32(KA, Qa, NM); SBAR();                                                            \
    X1(S, 0); RD0; SBAR(); CT(Pp1, 2, tl0, tl1); X1(S, 1); X1(S, 2);                            \
    Sn = MFMA32(KB_, Qb, Sn); SBAR();                                                           \
    X1(S, 3); RD1; SBAR(); CT(Pp1, 3, tl2, tl3); X1(S, 4); X1(S, 5);                            \
    O = MFMA32(VA, BC(Pp0), O); SBAR();                                                         \
    CV(Pn0, 0, S, 0); X1(S, 6); X2; SBAR(); X1(S, 7); CV(Pn0, 1, S, 2);                         \
    SUMP(L, Pp0); SBAR();                                                                       \
    X1(S, 8); X1(S, 9); CV(Pn0, 2, S, 4);                                                       \
    O = MFMA32(VB, BC(Pp1), O); SBAR();                                                         \
    X1(S, 10); X3; SBAR(); CV(Pn0, 3, S, 6); X1(S, 11); XT(tl0, S, 12);                         \
    SUMP(L, Pp1); SBAR();                                                                       \
    CV(Pn1, 0, S, 8); XT(tl1, S, 13); XT(tl2, S, 14); CV(Pn1, 1, S, 10); XT(tl3, S, 15);        \
  } while (0)
#define RECENTRE(NM, O, L, FE)                                                                  \
  do {                                                                                          \
    int e = __builtin_amdgcn_frexp_expf(L[0]) + (int)REF_BIAS;                                  \
    e = e > 0 ? e : 0;                                                                          \
    FE = (float)e;                                                                              \
    _Pragma("unroll") for (int r = 0; r < 16; ++r) { O[r] = __builtin_amdgcn_ldexpf(O[r], -e); NM[r] -= FE; } \
    _Pragma("unroll") for (int r = 0; r < 4; ++r) L[r] = __builtin_amdgcn_ldexpf(L[r], -e);     \
  } while (0)

constexpr int S_P1 = 0, S_P2 = 16, S_AT = 48, S_MG = 56, S_P4 = 57, S_P5 = 89, S_END = 105;

struct Ctx {
  const char *a1, *b1, *a2, *b2, *kv, *a4, *b4r, *b4i, *a5, *b5;
  unsigned lds0;
  int lw, lane;
};
template <int PH, int NW>
struct Loader {
  static constexpr int KA = PH <= 2 ? OP_ROW : OP_FRAG, KB = PH <= 2 ? OP_TR : PH == 4 ? OP_ROW : OP_TRP;
  static constexpr int LDA = PH == 1 ? 2048 : PH == 2 ? 4096 : PH == 4 ? 65536 : 32768, LDB = PH == 1 ? 6144 : 2048;
  static constexpr int NSUB = PH == 1 ? 3 : PH == 2 ? 2 : 1, SUBOFF = PH == 1 ? 2048 : 128;
  static constexpr int PA = 16 / NW, PB = 8 / NW;
  unsigned va[2], vb[2];
  __device__ __forceinline__ void init(const Ctx& c) {
#pragma unroll
    for (int j = 0; j < 2; ++j) {
      va[j] = PH == 3 ? c.lane * 16 : piece_off<KA>(c.lw * PA + j, c.lane, LDA);
      vb[j] = piece_off<KB>(c.lw * PB + j, c.lane, LDB);
    }
  }
  __device__ __forceinline__ void stage(const Ctx& c, int t, int s) const {
    const unsigned d = c.lds0 + (s & 3) * SLOT;
    if constexpr (PH == 3) {
#pragma unroll
      for (int h = 0; h < 4 / NW; ++h) {
        const int part = c.lw * (4 / NW) + h;
#pragma unroll
        for (int j = 0; j < 8; ++j)
          blds<1>(mkrs(c.kv), va[0], (part & 1) * (4 << 20) + (part >> 1) * 65536 + t * 8192 + j * 1024, d + part * 8192 + j * 1024);
      }
    } else {
      const char* pa = PH == 1 ? c.a1 : PH == 2 ? c.a2 : PH == 4 ? c.a4 : c.a5;
      const char* pb = PH == 1 ? c.b1 : PH == 2 ? c.b2 : PH == 4 ? (t < 16 ? c.b4r : c.b4i) : c.b5;
      const int sa = t * (KA == OP_ROW ? 128 : 2048), sb = PH == 4 ? (t & 15) * 128 : t * 64 * LDB;
#pragma unroll
      for (int j = 0; j < PA; ++j)
        blds<(PH >= 4 ? APOL : PH == 2 ? DNT : 0)>(mkrs(pa), va[j & 1], sa + (j >> 1) * (KA == OP_ROW ? 16 * LDA : LDA), d + (c.lw * PA + j) * 1024);
#pragma unroll
      for (int u = 0; u < NSUB; ++u)
#pragma unroll
        for (int j = 0; j < PB; ++j)
          blds<(PH == 1 ? DNT : 0)>(mkrs(pb), vb[j & 1], sb + u * SUBOFF + (j >> 1) * 16 * LDB, d + 16384 + u * 8192 + (c.lw * PB + j) * 1024);
    }
  }
};
__device__ __forceinline__ void touch(const void* p, unsigned voff, unsigned soff) {
  asm volatile("buffer_load_dword v255, %0, %1, %2 offen" ::"v"(voff), "s"(mkrs(p)), "s"(__builtin_amdgcn_readfirstlane(soff)) : "memory", "v255");
}

template <bool LD>
__device__ __forceinline__ void attention(const Ctx& c, const bf16x8 (&q)[4], const char* Qw, const char* Kh, const char* Vh, rsrc_t rO,
                                          unsigned ooff, int grp, int wq, const char* pU, gu32* uflags, gu32* myflag, unsigned tag, lptr smem) {
  const int lane = c.lane;
  const bf16x8 qa0 = q[0], qb0 = q[1], qa1 = q[2], qb1 = q[3];
  u32x4 ut;
  lptr usp = smem + wq * SLOT + 32768 + lane * 16;
#define URD(n) do { if constexpr (!LD) ut = *(const LAS u32x4*)(usp + (((t & 1) * 4 + (n)) << 10)); } while (0)
#define UST(n) do { if constexpr (!LD) st_wt(mkrs(pU, t < 2), lane * 16 + ((n) & 1) * 1024 + ((t & 1) * 2 + ((n) >> 1)) * 65536, ut); } while (0)
  Loader<3, 4> l3; Loader<4, 4> l4;
  if constexpr (LD) { l3.init(c); l4.init(c); }
  bf16x8 ones;
  {
    const __bf16 v = (((lane >> 2) ^ (lane >> 4)) & 1) ? (__bf16)0.0f : (__bf16)1.0f;
#pragma unroll
    for (int j = 0; j < 8; ++j) ones[j] = v;
  }
#define RDK(dst, P, n) dst = *(const LAS bf16x8*)((P) + (n) * 1024)
#define RDV(dst, P, n) dst = *(const LAS bf16x8*)((P) + 8192 + (n) * 1024)
  bf16x8 k0a, k0b, k1a, k1b, v0a, v0b, v1a, v1b;
  f32x16 sA, sB, nm0, nm1, o0 = {}, o1 = {};
  f32x4 l0 = {}, l1 = {};
  u32x4 pA0, pA1, pB0 = {}, pB1 = {};
  lptr kb = smem + grp * 16384 + lane * 16;
  int cur = (S_AT & 3) * SLOT;
  RDK(k0a, kb + cur, 0); RDK(k0b, kb + cur, 1); RDK(k1a, kb + cur, 2); RDK(k1b, kb + cur, 3);
  RDV(v0a, kb + cur, 0); RDV(v0b, kb + cur, 1);
  {
    const f32x16 z = {};
    f32x16 s0 = MFMA32(k0a, qa0, z); s0 = MFMA32(k0b, qb0, s0);
    f32x16 s1 = MFMA32(k1a, qa0, z); s1 = MFMA32(k1b, qb0, s1);
    nm0 = splat16(-(__builtin_ceilf(halfmax(__builtin_fmaxf(max16(s0), max16(s1)))) + REF_BIAS));
    s0 = MFMA32(k0a, qa1, z); s0 = MFMA32(k0b, qb1, s0);
    s1 = MFMA32(k1a, qa1, z); s1 = MFMA32(k1b, qb1, s1);
    nm1 = splat16(-(__builtin_ceilf(halfmax(__builtin_fmaxf(max16(s0), max16(s1)))) + REF_BIAS));
  }
  PIN(nm0); PIN(nm1);
  sA = MFMA32(k0a, qa0, nm0); sA = MFMA32(k0b, qb0, sA);
  v1a = v0a; v1b = v0a;
  STAMP(4);
  int t = 0;
  float tl0 = 0.f, tl1 = 0.f, tl2 = 0.f, tl3 = 0.f;
#define NOP (void)0
#define UE(KA, KB_, VA, VB, R0, R1, X2, X3) UNIT(sA, pA0, pA1, sB, KA, KB_, qa1, qb1, nm1, o1, l1, VA, VB, pB0, pB1, R0, R1, X2, X3)
#define UO(KA, KB_, VA, VB, R0, R1, X2, X3) UNIT(sB, pB0, pB1, sA, KA, KB_, qa0, qb0, nm0, o0, l0, VA, VB, pA0, pA1, R0, R1, X2, X3)
#pragma unroll 1
  while (t < 8) {
    bool trig;
#pragma unroll 1
    do {
      lptr cp = kb + cur;
      cur = cur == 3 * SLOT ? 0 : cur + SLOT;
      lptr np = kb + cur;
      SBAR();
      UE(k0a, k0b, v1a, v1b, RDK(k1a, cp, 2), RDK(k1b, cp, 3), URD(0), UST(0));
      UO(k1a, k1b, v0a, v0b, RDV(v1a, cp, 2), RDV(v1b, cp, 3), URD(1), UST(1));
      UE(k1a, k1b, v0a, v0b, RDK(k0a, cp, 4), RDK(k0b, cp, 5), URD(2), UST(2));
      UO(k0a, k0b, v1a, v1b, RDV(v0a, cp, 4), RDV(v0b, cp, 5), URD(3), UST(3));
      UE(k0a, k0b, v1a, v1b, RDK(k1a, cp, 6), RDK(k1b, cp, 7), NOP, NOP);
      UO(k1a, k1b, v0a, v0b, RDV(v1a, cp, 6), RDV(v1b, cp, 7), NOP, NOP);
      if constexpr (LD) {
        drain();
        if (t == 5 && wq == 2) await(uflags, 16, tag, lane, smem);
        bar_l();
        if (t < 4) l3.stage(c, t + 4, S_AT + t + 4);
        else if (t > 4) l4.stage(c, t - 5, S_P4 + t - 5);
        else if (wq == 2) mail(uflags, 16, lane, c.lds0);
        if (t == 2) set_flag(myflag, tag);
      } else {
        bar_vl<4>();
      }
      SBAR();
      UE(k1a, k1b, v0a, v0b, RDK(k0a, np, 0), RDK(k0b, np, 1), NOP, NOP);
      UO(k0a, k0b, v1a, v1b, RDV(v0a, np, 0), RDV(v0b, np, 1); trig = __any(l0[0] + l1[0] > L_LIMIT), NOP, NOP);
      ++t;
    } while (t < 8 && !trig);
    if (t >= 8) break;
    asm volatile("s_nop 7" : "+v"(l0), "+v"(l1));
    pB1[2] = pk(tl0, tl1); pB1[3] = pk(tl2, tl3);
    o1 = MFMA32(v1a, BC(pB0), o1); o1 = MFMA32(v1b, BC(pB1), o1);
    l1 = MFMA16(ones, BC(pB0), l1); l1 = MFMA16(ones, BC(pB1), l1);
    pB0 = u32x4{}; pB1 = u32x4{}; tl0 = 0.f; tl1 = 0.f; tl2 = 0.f; tl3 = 0.f;
    float f0, f1;
    RECENTRE(nm0, o0, l0, f0);
    RECENTRE(nm1, o1, l1, f1);
#pragma unroll
    for (int r = 0; r < 16; ++r) sA[r] -= f0;
    asm volatile("s_nop 1" : "+v"(l0), "+v"(l1), "+v"(pB0), "+v"(pB1));
  }
  STAMP(5);
  asm volatile("s_nop 7" : "+v"(l0), "+v"(l1));
  pB1[2] = pk(tl0, tl1); pB1[3] = pk(tl2, tl3);
  o1 = MFMA32(v1a, BC(pB0), o1); o1 = MFMA32(v1b, BC(pB1), o1);
  l1 = MFMA16(ones, BC(pB0), l1); l1 = MFMA16(ones, BC(pB1), l1);
  lptr mg = smem + (S_MG & 3) * SLOT + lane * 16;
  {
    const f32x16& og = LD ? o0 : o1;
    lptr d = mg + ((1 - grp) * 4 + wq) * 5120;
#pragma unroll
    for (int q = 0; q < 4; ++q) *(LAS f32x4*)(d + q * 1024) = f32x4{og[4 * q], og[4 * q + 1], og[4 * q + 2], og[4 * q + 3]};
    *(LAS f32x4*)(d + 4096) = f32x4{LD ? l0[0] : l1[0], LD ? nm0[0] : nm1[0], 0.f, 0.f};
  }
  bar_l();
  f32x16 o = LD ? o1 : o0;
  float l = LD ? l1[0] : l0[0];
  {
    lptr d = mg + (grp * 4 + wq) * 5120;
    const f32x4 e = *(const LAS f32x4*)(d + 4096);
    const float nmk = LD ? nm1[0] : nm0[0], nmin = __builtin_fminf(nmk, e[1]);
    const int ek = (int)__builtin_fmaxf(nmin - nmk, -400.f), eo = (int)__builtin_fmaxf(nmin - e[1], -400.f);
    l = __builtin_amdgcn_ldexpf(l, ek) + __builtin_amdgcn_ldexpf(e[0], eo);
#pragma unroll
    for (int q = 0; q < 4; ++q) {
      const f32x4 x = *(const LAS f32x4*)(d + q * 1024);
#pragma unroll
      for (int r = 0; r < 4; ++r) o[4 * q + r] = __builtin_amdgcn_ldexpf(o[4 * q + r], ek) + __builtin_amdgcn_ldexpf(x[r], eo);
    }
  }
  float chk = l;
#pragma unroll
  for (int r = 0; r < 16; ++r) chk += __builtin_fabsf(o[r]);
  u32x4 x, y;
  if (__builtin_expect(__any(!(chk < 3.0e38f) || !(l > 0.f)), 0)) attn_safe(Kh, Vh, Qw + grp * 2048, lane, x, y);
  else pack16(o, 1.0f / l, x, y);
  const unsigned vo = ooff + grp * 65536 + ((lane >> 4) & 1) * 32768 + (lane >> 5) * 256 + (lane & 15) * 16;
  st_wt(rO, vo, x); st_wt(rO, vo + 512, y);
}

__device__ __forceinline__ void store_out(const f32x4 (&acc)[4][2], u16* out, int row0, int col0, int lane) {
#pragma unroll
  for (int mi = 0; mi < 4; ++mi)
    *(u32x4*)(out + (row0 + mi * 16 + (lane & 15)) * 2048 + col0 + (lane >> 4) * 8) = natural(pack2(acc[mi][0], acc[mi][1], 1.0f));
}

__global__ __launch_bounds__(512, 2) void fused(const u16* __restrict__ xA, const u16* __restrict__ Wsr, const u16* __restrict__ Wsi,
                                                const u16* __restrict__ Wdr, const u16* __restrict__ Wdi, const u16* __restrict__ xB,
                                                const u16* __restrict__ wqkv, const u16* __restrict__ wo, u16* __restrict__ out,
                                                char* ws) {
  extern __shared__ __attribute__((aligned(16))) char smem_[];
  lptr smem = (lptr)smem_;
  const char* pc;
  asm volatile("s_getpc_b64 %0" : "=s"(pc));
  const int lane = threadIdx.x & 63, w = __builtin_amdgcn_readfirstlane(threadIdx.x >> 6), wm = (w >> 1) & 1, wn = w & 1;
  const int xcd = blockIdx.x & 7, i = blockIdx.x >> 3;
  gu32* flags = (gu32*)ws + 64;
  char* QKV = ws + (1 << 20);
  char* Of = QKV + (12 << 20);
  char* Uf = Of + (4 << 20);
  const int j2 = xcd * 2 + (i >> 4), tb1 = i & 15;
  const int tbA = xcd * 2 + (i >> 4), im = (i >> 3) & 1, nb2 = i & 7, eb = i & 15;
  const int head = xcd * 4 + (i >> 3), qb = i & 7;
  const int tb5 = (xcd >> 1) * 4 + (i & 3), nb5 = (xcd & 1) * 8 + (i >> 2);
  const char* Kh = QKV + (4 << 20) + head * 131072;
  const char* Vh = QKV + (8 << 20) + head * 131072;
  const int qt = qb * 8 + (w & 3) * 2;
  const char* Qw = QKV + (head * 64 + qt) * 2048;
  const unsigned ooff = qt * 65536 + head * 1024;
  Ctx c;
  c.lane = lane; c.lw = w; c.lds0 = (unsigned)(unsigned long)smem;
  c.a1 = (const char*)(xB + tb1 * 128 * D_); c.b1 = (const char*)(wqkv + j2 * 64);
  ST3(64, 109);
  {
    Loader<1, 8> l0; l0.init(c);
    l0.stage(c, 0, 0);
  }
  const void* p1 = w == 7 ? (const void*)c.b1 : (const void*)(xB + blockIdx.x * 8 * D_);
  const unsigned vo1 = w == 7 ? (lane < 48 ? (tb1 * 16 + (lane & 15)) * 6144 + (lane >> 4) * 2048 : 0u) : ((lane >> 2) & 7) * 2048 + (lane & 3) * 128;
  const unsigned qs1 = w == 7 ? 256 * 6144 : 512;
  if (w >= 6) {
#pragma unroll
    for (int k = 0; k < NQE; ++k) touch(p1, vo1, k * qs1);
  }
  SBAR();
  ST3(64, 112); ST3(256, 113); ST3(384, 114);
  c.lw = w & 3;
  STAMP(0);
  if (__builtin_expect(w >= 4, LK)) {
    c.a2 = (const char*)((im ? Wsi : Wsr) + tbA * 128 * S_); c.b2 = (const char*)(xA + nb2 * 128);
    c.kv = Kh;
    c.a4 = Uf + tbA * 8 * 65536; c.b4r = (const char*)(Wdr + eb * 64 * D_); c.b4i = (const char*)(Wdi + eb * 64 * D_);
    c.a5 = Of + tb5 * 8 * 32768; c.b5 = (const char*)(wo + nb5 * 64);
    gu32 *fq = flags + (head >> 1) * 16, *fo = flags + 512 + (tb5 >> 1) * 32;
    unsigned tag = 0;
    bf16x8 q[4];
    auto load_q = [&]() {
#pragma unroll
      for (int k = 0; k < 4; ++k) blds<1>(mkrs(Qw), lane * 16, k * 1024, c.lds0 + c.lw * SLOT + 32768 + k * 1024);
    };
    auto read_q = [&]() {
#pragma unroll
      for (int k = 0; k < 4; ++k) q[k] = *(const LAS bf16x8*)(smem + c.lw * SLOT + 32768 + k * 1024 + lane * 16);
    };
    if (__builtin_expect(w < 6, 1)) {
      {
        ST3(256, 110);
        Loader<1, 2> l1; l1.init(c);
        l1.stage(c, 1, 1);
        ST3(256, 111);
#pragma unroll 1
        for (int s = -1; s <= CUP; ++s) { bar_v<20>(); l1.stage(c, s + 3, s + 3); }
        l1.stage(c, CUP + 4, CUP + 4);
#pragma unroll 1
        for (int s = CUP + 1; s < 12; ++s) { bar_v<40>(); l1.stage(c, s + 4, s + 4); }
      }
      {
        Loader<2, 2> l2; l2.init(c);
#pragma unroll 1
        for (int s = 12; s < 44; ++s) { bar_v<32>(); l2.stage(c, s - 12, s + 4); }
      }
      Loader<3, 2> l3; l3.init(c);
      bar_v<32>();
      load_q();
      l3.stage(c, 0, 48);
#pragma unroll 1
      for (int s = 45; s < 47; ++s) { bar_v<32>(); l3.stage(c, s - 44, s + 4); }
      asm volatile("s_waitcnt vmcnt(32)" ::: "memory");
      read_q();
      bar_l();
      l3.stage(c, 3, 51);
    } else {
      const bool u = w == 7;
      const int b = blockIdx.x;
      ST3(384, 115);
      const unsigned vo2 = (lane >> 2) * 4096 + (lane & 3) * 128;
      const char* pKVo = QKV + (4 << 20) + ((j2 * 2) * 64 + tb1 * 4 + (w & 1) * 2) * 2048;
      bar_v<NQE>();
#pragma unroll 1
      for (int s = 0; s < 44; ++s) {
        if (s == 27) drain();
        bar_l();
#pragma unroll
        for (int k = NQE; k < 4; ++k)
          if (s == QS + (k - NQE) * QI) touch(p1, vo1, k * qs1);
        if (s == CS) {
          if (u) {
#pragma unroll
            for (int k = 0; k < CODEPF; ++k) touch(pc, lane * 128, k * 8192);
          } else touch(ws, 0, 0);
        }
        if (s == 12) {
          asm volatile("s_load_dword %0, %1, 0x0\n\ts_waitcnt lgkmcnt(0)" : "=s"(tag) : "s"(ws) : "memory");
          tag = tag * 1664525u + 1013904223u;
        }
        if (s == PWS + PWI * nb2) {
#pragma unroll
          for (int k = 0; k < 3; ++k) touch(c.a2 + nb2 * 512, vo2, (k + (u ? 5 : 0)) * 65536);
#pragma unroll
          for (int k = 0; k < 2; ++k) {
            if (u) touch(xA + (nb2 * 256 + head * 8) * D_, lane * 128, k * 8192);
            else touch(c.a2 + nb2 * 512, vo2, (k + 3) * 65536);
          }
        }
        if (s >= 16 && s < 24) {
          const int n = s - 16;
#pragma unroll
          for (int h = 0; h < 2; ++h) {
            const u32x4 v = *(const LAS u32x4*)(smem + ((w & 1) * 2 + h) * SLOT + 32768 + n * 1024 + lane * 16);
            __builtin_amdgcn_raw_buffer_store_b128(v, mkrs(pKVo), lane * 16, h * 131072 + (n >> 2) * (4 << 20) + (n & 3) * 1024, 16);
          }
        }
        if (s == 27) set_flag(flags + j2 * 16 + tb1, tag);
        if (s == 40 && !u) mail(fq, 16, lane, c.lds0);
      }
      drain();
      if (!u) await(fq, 16, tag, lane, smem);
      bar_l();
      load_q();
      bar_l(); bar_l();
      drain();
      read_q();
      bar_l();
      touch(u ? Wdi : Wdr, lane * 128, b * 8192);
      touch(wo, (lane & 31) * 128, b * 8192 + (u ? 4096 : 0));
    }
    STAMP(3);
    attention<true>(c, q, Qw, Kh, Vh, mkrs(Of), ooff, 1, w & 3, nullptr, flags + 1024 + tbA * 16, flags + 1024 + tbA * 16 + (i & 15), tag, smem);
    STAMP(6);
    bar_vl<2>();
    if (w < 6) {
      {
        Loader<4, 2> l4; l4.init(c);
        l4.stage(c, 3, S_P4 + 3);
#pragma unroll 1
        for (int s = S_P4; s < S_P4 + 2; ++s) { bar_v<26>(); l4.stage(c, s + 4 - S_P4, s + 4); }
#pragma unroll 1
        for (int s = S_P4 + 2; s < S_P5 - 4; ++s) { bar_v<24>(); l4.stage(c, s + 4 - S_P4, s + 4); }
      }
      Loader<5, 2> l5; l5.init(c);
#pragma unroll 1
      for (int s = S_P5 - 4; s < S_END - 4; ++s) { bar_v<24>(); l5.stage(c, s + 4 - S_P5, s + 4); }
      bar_v<24>(); bar_v<12>(); bar_v<0>(); bar_v<0>();
    } else {
#pragma unroll 1
      for (int s = S_P4; s < S_P5 - 4; ++s) {
        if (s == S_P4 + 4) drain();
        bar_l();
        if (s == S_P4 + 4) set_flag(flags + 512 + qb * 32 + head, tag);
        if (s == S_P5 - 8 && w == 6) mail(fo, 32, lane, c.lds0);
        if (blockIdx.x == 0 && w == 6) {
          if (s == S_P4 + 8) blds<1>(mkrs((const void*)flags), lane * 16, 0, c.lds0 + MAIL + 1024);
          if (s == S_P4 + 16) {
            drain();
            u32x4 f = *(volatile LAS u32x4*)(smem + MAIL + 1024 + lane * 16);
#pragma unroll 1
            for (unsigned spins = 0; spins < 400000u && !__all(f[0] == tag && f[1] == tag && f[2] == tag && f[3] == tag); ++spins) {
              __builtin_amdgcn_s_sleep(2);
              f = __builtin_amdgcn_raw_buffer_load_b128(mkrs((const void*)flags), lane * 16, 0, 16);
            }
            set_flag((gu32*)ws, tag);
          }
        }
      }
      drain();
      if (w == 6) await(fo, 32, tag, lane, smem);
      bar_l();
    }
    return;
  }
  const unsigned lds0 = c.lds0;
  lptr park = smem + w * SLOT + 32768;
  ST3(64, 116);
  ST3(64, 117);
  bar_v<0>();
  ST3(64, 118);
  {
    FragRd<OP_ROW, 1> ra; ra.init(wm * 4, lane, lds0 + (S_P1 & 3) * SLOT);
    FragRd<OP_TR, 2> rb; rb.init(wn * 2, lane, lds0 + (S_P1 & 3) * SLOT + 16384);
    f32x4 acc[4][6] = {};
    consume<4, 6, 4, S_P1, 16, 0>(acc, ra, rb);
    STAMP(1);
    const rsrc_t rQ = mkrs(QKV + ((j2 * 2 + wn) * 64 + tb1 * 4 + wm * 2) * 2048);
    const int cc = lane & 15, g = lane >> 4;
#pragma unroll
    for (int mi = 0; mi < 4; ++mi) {
      const unsigned blk = (mi >> 1) * 2048 + g * 512;
      st_wt(rQ, blk + ((mi & 1) * 16 + cc) * 16, pack2(acc[mi][0], acc[mi][1], 0.17677669529663687f * 1.4426950408889634f));
      *(LAS u32x4*)(park + blk + ((cc >> 3) * 16 + (mi & 1) * 8 + (cc & 7)) * 16) = pack2(acc[mi][2], acc[mi][3], 1.0f);
      *(LAS u32x4*)(park + 4096 + blk + ((mi & 1) * 16 + cc) * 16) = pack2(acc[mi & 2][4 + (mi & 1)], acc[(mi & 2) + 1][4 + (mi & 1)], 1.0f);
    }
    STAMP(2);
  }
  bf16x8 q[4];
  {
    FragRd<OP_ROW, 1> ra; ra.init(wm * 4, lane, lds0 + (S_P2 & 3) * SLOT);
    FragRd<OP_TR, 4> rb; rb.init(0, lane, lds0 + (S_P2 & 3) * SLOT + 16384 + wn * 8192);
    f32x4 acc[4][4] = {};
    consume<4, 4, 4, S_P2, 32, 11>(acc, ra, rb);
    STAMP(3);
#pragma unroll
    for (int k = 0; k < 4; ++k) q[k] = *(const LAS bf16x8*)(park + k * 1024 + lane * 16);
    const float sc = im ? -1.0f : 1.0f;
#pragma unroll
    for (int mi = 0; mi < 4; ++mi)
#pragma unroll
      for (int n2 = 0; n2 < 2; ++n2)
        *(LAS u32x4*)(park + (mi * 2 + n2) * 1024 + lane * 16) = natural(pack2(acc[mi][2 * n2], acc[mi][2 * n2 + 1], sc));
  }
  attention<false>(c, q, Qw, Kh, Vh, mkrs(Of), ooff, 0, w, Uf + (tbA * 8 + wm * 4) * 65536 + (im * 32 + nb2 * 4 + wn * 2) * 1024,
                   nullptr, nullptr, 0u, smem);
  STAMP(6);
  bar_l();
  {
    FragRd<OP_FRAG, 1> ra; ra.init(wm * 4, lane, lds0 + (S_P4 & 3) * SLOT);
    FragRd<OP_ROW, 1> rb; rb.init(wn * 2, lane, lds0 + (S_P4 & 3) * SLOT + 16384);
    f32x4 acc[4][2] = {};
    consume<4, 2, 2, S_P4, 32, 4>(acc, ra, rb);
    STAMP(7);
    store_out(acc, out, tbA * 128 + wm * 64, eb * 64 + wn * 32, lane);
  }
  {
    FragRd<OP_FRAG, 1> ra; ra.init(wm * 4, lane, lds0 + (S_P5 & 3) * SLOT);
    FragRd<OP_TRP, 2> rb; rb.init(wn * 2, lane, lds0 + (S_P5 & 3) * SLOT + 16384);
    f32x4 acc[4][2] = {};
    consume<4, 2, 2, S_P5, 16, 0>(acc, ra, rb);
    STAMP(8);
    store_out(acc, out, tb5 * 128 + wm * 64, 1024 + nb5 * 64 + wn * 32, lane);
  }
}

extern "C" void kernel_launch(void* const* d_in, const int* in_sizes, int n_in, void* d_out, int out_size, void* d_ws,
                              size_t ws_size, hipStream_t stream) {
  (void)hipFuncSetAttribute((const void*)fused, hipFuncAttributeMaxDynamicSharedMemorySize, LDS_BYTES);
  fused<<<256, 512, LDS_BYTES, stream>>>((const u16*)d_in[0], (const u16*)d_in[1], (const u16*)d_in[2], (const u16*)d_in[3],
                                         (const u16*)d_in[4], (const u16*)d_in[5], (const u16*)d_in[6], (const u16*)d_in[7],
                                         (u16*)d_out, (char*)d_ws);
}
```

```cpp
#include <hip/hip_runtime.h>
#include <hip/hip_bf16.h>
#include <type_traits>

typedef unsigned short u16;
typedef __attribute__((ext_vector_type(8))) __bf16 bf16x8;
typedef __attribute__((ext_vector_type(2))) __bf16 bf16x2;
typedef __attribute__((ext_vector_type(2))) float f32x2;
typedef __attribute__((ext_vector_type(4))) float f32x4;
typedef __attribute__((ext_vector_type(16))) float f32x16;
typedef __attribute__((ext_vector_type(4))) unsigned u32x4;
typedef __attribute__((ext_vector_type(2))) unsigned u32x2;
typedef __attribute__((ext_vector_type(4))) short s16x4;
#define LAS __attribute__((address_space(3)))
#define GAS __attribute__((address_space(1)))
typedef LAS char* lptr;
typedef GAS unsigned gu32;
typedef __amdgpu_buffer_rsrc_t rsrc_t;

#define SBAR() __builtin_amdgcn_sched_barrier(0)
#define PIN(x) asm volatile("" : "+v"(x))
#define MFMA32(a, b, c) __builtin_amdgcn_mfma_f32_32x32x16_bf16((a), (b), (c), 0, 0, 0)
#define MFMA16(a, b, c) __builtin_amdgcn_mfma_f32_16x16x32_bf16((a), (b), (c), 0, 0, 0)
#define BC(x) __builtin_bit_cast(bf16x8, x)
#define RLX_AGENT __ATOMIC_RELAXED, __HIP_MEMORY_SCOPE_AGENT
template <int N> __device__ __forceinline__ void bar_vl() { asm volatile("s_waitcnt vmcnt(%0) lgkmcnt(0)\n\ts_barrier" ::"n"(N) : "memory"); }

#define STAMP(I)
#define ST4(I)
#define ST2(W, I)
#define ST3(T, I)

#define APOL 1
#define CUP 0
#define CODEPF 0
#define NQE 2
#define KVN 4
#define TP 4
#define FL (KVN > 8 ? 34 : 27)
#define OFL 3
#define TU -1
#define FULL 1
#define LK 1
#define QS 2
#define QI 0
#define CS 2
#define DNT 0
#define PWS 2
#define PWI 3
#define PFS 2
#define PFI 3
constexpr int S_ = 2048, D_ = 1024, SLOT = 40960, LDS_BYTES = 4 * SLOT;
enum { OP_ROW = 0, OP_FRAG = 1, OP_TR = 2, OP_TRP = 3 };

__device__ __forceinline__ unsigned pk(float lo, float hi) {
  f32x2 v = {lo, hi};
  return __builtin_bit_cast(unsigned, __builtin_convertvector(v, bf16x2));
}
__device__ __forceinline__ rsrc_t mkrs(const void* p, bool on = true) {
  return __builtin_amdgcn_make_buffer_rsrc((void*)p, 0, on ? 0x7fffffff : 0, 0x00020000);
}
template <int POL = 0>
__device__ __forceinline__ void blds(rsrc_t r, unsigned voff, unsigned soff, unsigned l) {
  if constexpr (POL == 1) asm volatile("s_mov_b32 m0, %0\n\ts_nop 0\n\tbuffer_load_dwordx4 %1, %2, %3 offen sc1 lds" ::"s"(l), "v"(voff), "s"(r), "s"(soff) : "memory");
  else if constexpr (POL == 2) asm volatile("s_mov_b32 m0, %0\n\ts_nop 0\n\tbuffer_load_dwordx4 %1, %2, %3 offen nt lds" ::"s"(l), "v"(voff), "s"(r), "s"(soff) : "memory");
  else asm volatile("s_mov_b32 m0, %0\n\ts_nop 0\n\tbuffer_load_dwordx4 %1, %2, %3 offen lds" ::"s"(l), "v"(voff), "s"(r), "s"(soff) : "memory");
}
__device__ __forceinline__ bf16x8 ld_dev(rsrc_t r, unsigned voff, unsigned soff = 0) {
  return __builtin_bit_cast(bf16x8, __builtin_amdgcn_raw_buffer_load_b128(r, voff, soff, 16));
}
__device__ __forceinline__ void st_wt(rsrc_t r, unsigned voff, u32x4 v) { __builtin_amdgcn_raw_buffer_store_b128(v, r, voff, 0, 16); }

template <int KIND>
__device__ __forceinline__ unsigned piece_off(int p, int i, int ld) {
  if constexpr (KIND == OP_ROW) return (8 * p + (i >> 3)) * ld + (((i & 7) ^ (((p & 1) << 2) | (i >> 4))) << 4);
  else if constexpr (KIND == OP_FRAG) return (p >> 1) * ld + (p & 1) * 1024 + i * 16;
  else if constexpr (KIND == OP_TR) return (8 * p + (i >> 3)) * ld + (((i & 7) ^ ((((i >> 4) & 1) | ((p & 1) << 1)) << 1)) << 4);
  else return (8 * p + (i >> 3)) * ld + (((i & 7) ^ ((i >> 4) << 1)) << 4);
}
template <int KIND, int NBASE>
struct FragRd {
  static constexpr int NB = KIND == OP_ROW ? 2 : (KIND == OP_FRAG ? 1 : NBASE);
  static constexpr int OPS = KIND >= OP_TR ? 2 : 1;
  unsigned base[NB];
  __device__ __forceinline__ void init(int b0, int lane, unsigned img) {
    const int r = lane & 15, g = lane >> 4;
    if constexpr (KIND == OP_ROW) {
#pragma unroll
      for (int ks = 0; ks < 2; ++ks) base[ks] = img + (b0 * 16 + r) * 128 + (((4 * ks + g) ^ ((r >> 1) & 7)) << 4);
    } else if constexpr (KIND == OP_FRAG) {
      base[0] = img + b0 * 2048 + lane * 16;
    } else {
      const int q = r >> 2, p = lane & 3, f = ((q >> 1) & 1) | ((g & 1) << 1);
      const int row = (KIND == OP_TR ? 8 * g : 16 * (g >> 1) + 4 * (g & 1)) + q;
#pragma unroll
      for (int b = 0; b < NBASE; ++b) base[b] = img + row * 128 + (((((b0 + b) ^ f) << 1) | (p >> 1)) << 4) + (p & 1) * 8;
    }
  }
  __device__ __forceinline__ void step(int k, int d) { base[k] += d; PIN(base[k]); }
  __device__ __forceinline__ void rd(u32x4& f, int b, int ks, int half) const {
    if constexpr (KIND == OP_ROW) f = *(const LAS u32x4*)(unsigned long)(base[ks] + b * 2048);
    else if constexpr (KIND == OP_FRAG) f = *(const LAS u32x4*)(unsigned long)(base[0] + b * 2048 + ks * 1024);
    else {
      const u32x2 x = __builtin_bit_cast(u32x2, __builtin_amdgcn_ds_read_tr16_b64_v4i16(
          (LAS s16x4*)(unsigned long)(base[b % NBASE] + (b / NBASE) * 8192 + ks * 4096 + half * (KIND == OP_TR ? 512 : 1024))));
      f[2 * half] = x[0]; f[2 * half + 1] = x[1];
    }
  }
};

__device__ __forceinline__ u32x4 pack2(const f32x4& x, const f32x4& y, float sc) {
  return u32x4{pk(x[0] * sc, x[1] * sc), pk(x[2] * sc, x[3] * sc), pk(y[0] * sc, y[1] * sc), pk(y[2] * sc, y[3] * sc)};
}
__device__ __forceinline__ u32x4 natural(u32x4 c) {
  auto x = __builtin_amdgcn_permlane32_swap(c[0], c[2], false, false);
  auto y = __builtin_amdgcn_permlane32_swap(c[1], c[3], false, false);
  auto p = __builtin_amdgcn_permlane16_swap(x[0], x[1], false, false);
  auto q = __builtin_amdgcn_permlane16_swap(y[0], y[1], false, false);
  return u32x4{p[0], q[0], p[1], q[1]};
}

__device__ __forceinline__ void bar_l() { asm volatile("s_waitcnt lgkmcnt(0)\n\ts_barrier" ::: "memory"); }
template <int N> __device__ __forceinline__ void bar_v() { asm volatile("s_waitcnt vmcnt(%0)\n\ts_barrier" ::"n"(N) : "memory"); }
__device__ __forceinline__ void drain() { asm volatile("s_waitcnt vmcnt(0)" ::: "memory"); }
__device__ __forceinline__ void set_flag(gu32* flag, unsigned tag) {
  if (threadIdx.x == 384) __hip_atomic_store(flag, tag, RLX_AGENT);
}
constexpr int MAIL = 32768 + 4096;
__device__ __forceinline__ void mail(gu32* flags, int n, int lane, unsigned lds0) {
  asm volatile("s_mov_b32 m0, %0\n\ts_nop 0\n\tbuffer_load_dword %1, %2, 0 offen sc1 lds"
               ::"s"(lds0 + MAIL), "v"((lane < n ? lane : 0) * 4), "s"(mkrs((const void*)flags)) : "memory");
}
__device__ __forceinline__ void await(gu32* flags, int n, unsigned tag, int lane, lptr smem) {
  if (__builtin_expect(!__all(*(volatile LAS unsigned*)(smem + MAIL + lane * 4) == tag), 0)) {
#pragma unroll 1
    for (unsigned spins = 0; spins < 400000u; ++spins) {
      if (__all(__hip_atomic_load(flags + (lane < n ? lane : 0), RLX_AGENT) == tag)) break;
      __builtin_amdgcn_s_sleep(2);
    }
  }
  __builtin_amdgcn_fence(__ATOMIC_ACQUIRE, "wavefront");
}
#define WAIT_LGKM(n) __builtin_amdgcn_s_waitcnt(0xC07F | ((n) << 8))

template <int MI, int NI, int NSWAP, int S0, int NT, int ND, class RA, class RB>
__device__ __forceinline__ void consume(f32x4 (&acc)[MI][NI], RA& ra, RB& rb) {
  u32x4 a0[MI], b0[NI], a1[MI], b1[NI];
  constexpr int NM = MI * NI, NBO = NI * RB::OPS, NR = MI + NBO, NU = RA::NB + RB::NB;
  auto rdop = [&](u32x4(&na)[MI], u32x4(&nb)[NI], int ks, int z) {
    if (z == 0) ra.rd(na[0], 0, ks, 0);
    else if (z <= NBO) rb.rd(nb[(z - 1) / RB::OPS], (z - 1) / RB::OPS, ks, (z - 1) % RB::OPS);
    else ra.rd(na[z - NBO], z - NBO, ks, 0);
  };
  auto mma = [&](const u32x4(&fa)[MI], const u32x4(&fb)[NI], int i) {
    const int mi = i / NI, ni = i % NI;
    acc[mi][ni] = ni < NSWAP ? MFMA16(BC(fb[ni]), BC(fa[mi]), acc[mi][ni]) : MFMA16(BC(fa[mi]), BC(fb[ni]), acc[mi][ni]);
    SBAR();
  };
#pragma unroll
  for (int z = 0; z < NR; ++z) { rdop(a0, b0, 0, z); SBAR(); }
  int slot = S0 & 3;
#pragma unroll 1
  for (int seg = ND ? 0 : 1; seg < 2; ++seg) {
#pragma unroll 1
    for (int t = seg ? NT - ND : ND; t > 0; --t) {
      slot = (slot + 1) & 3;
      const int d = slot == 0 ? -3 * SLOT : SLOT;
      WAIT_LGKM(MI - 1);
      SBAR();
#pragma unroll
      for (int i = 0; i < NM + 2; ++i) {
        if (i < NM) mma(a0, b0, i);
        else { if (i == NM) { WAIT_LGKM(0); SBAR(); } mma(a1, b1, i - NM); }
        if (i < NR) rdop(a1, b1, 1, i);
        else {
          constexpr int G = NM + 2 - NR;
#pragma unroll
          for (int u = (i - NR) * NU / G; u < (i - NR + 1) * NU / G; ++u) { if (u < RA::NB) ra.step(u, d); else rb.step(u - RA::NB, d); }
        }
        SBAR();
      }
      ST2(0, S0 + (seg ? NT - t : ND - t));
      asm volatile("s_barrier" ::: "memory");
      ST2(1, S0 + (seg ? NT - t : ND - t));
      SBAR();
      constexpr int G1 = NM - 2;
#pragma unroll
      for (int i = 0; i < G1; ++i) {
        if (NR > G1 && i < NR - G1) { rdop(a0, b0, 0, 2 * i); SBAR(); }
        mma(a1, b1, i + 2);
        const int z = NR > G1 ? (i < NR - G1 ? 2 * i + 1 : i + NR - G1) : i;
        if (z < NR) rdop(a0, b0, 0, z);
        SBAR();
      }
    }
    if (ND) drain();
  }
}

__device__ __forceinline__ void pack16(const f32x16& a, float sc, u32x4& c0, u32x4& c1) {
  c0 = u32x4{pk(a[0] * sc, a[1] * sc), pk(a[2] * sc, a[3] * sc), pk(a[4] * sc, a[5] * sc), pk(a[6] * sc, a[7] * sc)};
  c1 = u32x4{pk(a[8] * sc, a[9] * sc), pk(a[10] * sc, a[11] * sc), pk(a[12] * sc, a[13] * sc), pk(a[14] * sc, a[15] * sc)};
}
__device__ __forceinline__ float halfmax(float m) {
  auto rr = __builtin_amdgcn_permlane32_swap(__float_as_uint(m), __float_as_uint(m), false, false);
  return __builtin_fmaxf(__uint_as_float(rr[0]), __uint_as_float(rr[1]));
}
__device__ __forceinline__ float max16(const f32x16& s) {
  float a = __builtin_fmaxf(s[0], s[1]);
#pragma unroll
  for (int r = 2; r < 16; ++r) a = __builtin_fmaxf(a, s[r]);
  return a;
}
__device__ __forceinline__ f32x16 splat16(float v) {
  f32x16 r;
#pragma unroll
  for (int i = 0; i < 16; ++i) r[i] = v;
  return r;
}
#define RBIAS 96.0f
constexpr float REF_BIAS = RBIAS;
constexpr float L_LIMIT = 1.099511627776e12f;

__device__ __forceinline__ void attn_safe(const char* Kh, const char* Vh, const char* Qt, int lane, u32x4& c0, u32x4& c1) {
  const rsrc_t rq = mkrs(Qt), rk = mkrs(Kh), rv = mkrs(Vh);
  const bf16x8 q0 = ld_dev(rq, lane * 16), q1 = ld_dev(rq, 1024 + lane * 16);
  float m = -3.0e38f, l = 0.f;
  f32x16 o = {};
  const f32x16 z = {};
#pragma unroll 1
  for (int kt = 0; kt < 64; ++kt) {
    const bf16x8 k0 = ld_dev(rk, lane * 16, kt * 2048), k1 = ld_dev(rk, 1024 + lane * 16, kt * 2048);
    const bf16x8 v0 = ld_dev(rv, lane * 16, kt * 2048), v1 = ld_dev(rv, 1024 + lane * 16, kt * 2048);
    f32x16 s = MFMA32(k0, q0, z);
    s = MFMA32(k1, q1, s);
    const float mn = __builtin_fmaxf(m, halfmax(max16(s)));
    const float al = __builtin_amdgcn_exp2f(m - mn);
    m = mn;
    float rs = 0.f;
#pragma unroll
    for (int r = 0; r < 16; ++r) { s[r] = __builtin_amdgcn_exp2f(s[r] - mn); o[r] *= al; }
    u32x4 p0, p1;
    pack16(s, 1.0f, p0, p1);
#pragma unroll
    for (int r = 0; r < 4; ++r) {
      rs += __uint_as_float(p0[r] << 16) + __uint_as_float(p0[r] & 0xffff0000u);
      rs += __uint_as_float(p1[r] << 16) + __uint_as_float(p1[r] & 0xffff0000u);
    }
    l = l * al + rs;
    o = MFMA32(v0, BC(p0), o);
    o = MFMA32(v1, BC(p1), o);
  }
  auto rr = __builtin_amdgcn_permlane32_swap(__float_as_uint(l), __float_as_uint(l), false, false);
  l = __uint_as_float(rr[0]) + __uint_as_float(rr[1]);
  pack16(o, 1.0f / l, c0, c1);
}

#define EXP2(x) __builtin_amdgcn_exp2f(x)
#define X1(S, i) do { S[i] = EXP2(S[i]); PIN(S[i]); SBAR(); } while (0)
#define XT(T, S, i) do { T = EXP2(S[i]); PIN(T); SBAR(); } while (0)
#define CV(P, j, S, i) do { P[j] = pk(S[i], S[i + 1]); PIN(P[j]); SBAR(); } while (0)
#define CT(P, j, A, B) do { P[j] = pk(A, B); PIN(P[j]); SBAR(); } while (0)
#define SUMP(L, P) asm volatile("v_mfma_f32_16x16x32_bf16 %0, %1, %2, %0" : "+v"(L) : "v"(ones), "v"(P))
#define UNIT(S, Pn0, Pn1, Sn, KA, KB_, Qa, Qb, NM, O, L, VA, VB, Pp0, Pp1, RD0, RD1, X2, X3)     \
  do {                                                                                          \
    Sn = MFMA32(KA, Qa, NM); SBAR();                                                            \
    X1(S, 0); RD0; SBAR(); CT(Pp1, 2, tl0, tl1); X1(S, 1); X1(S, 2);                            \
    Sn = MFMA32(KB_, Qb, Sn); SBAR();                                                           \
    X1(S, 3); RD1; SBAR(); CT(Pp1, 3, tl2, tl3); X1(S, 4); X1(S, 5);                            \
    O = MFMA32(VA, BC(Pp0), O); SBAR();                                                         \
    CV(Pn0, 0, S, 0); X1(S, 6); X2; SBAR(); X1(S, 7); CV(Pn0, 1, S, 2);                         \
    SUMP(L, Pp0); SBAR();                                                                       \
    X1(S, 8); X1(S, 9); CV(Pn0, 2, S, 4);                                                       \
    O = MFMA32(VB, BC(Pp1), O); SBAR();                                                         \
    X1(S, 10); X3; SBAR(); CV(Pn0, 3, S, 6); X1(S, 11); XT(tl0, S, 12);                         \
    SUMP(L, Pp1); SBAR();                                                                       \
    CV(Pn1, 0, S, 8); XT(tl1, S, 13); XT(tl2, S, 14); CV(Pn1, 1, S, 10); XT(tl3, S, 15);        \
  } while (0)
#define RECENTRE(NM, O, L, FE)                                                                  \
  do {                                                                                          \
    int e = __builtin_amdgcn_frexp_expf(L[0]) + (int)REF_BIAS;                                  \
    e = e > 0 ? e : 0;                                                                          \
    FE = (float)e;                                                                              \
    _Pragma("unroll") for (int r = 0; r < 16; ++r) { O[r] = __builtin_amdgcn_ldexpf(O[r], -e); NM[r] -= FE; } \
    _Pragma("unroll") for (int r = 0; r < 4; ++r) L[r] = __builtin_amdgcn_ldexpf(L[r], -e);     \
  } while (0)

constexpr int S_P1 = 0, S_P2 = 16, S_AT = 48, S_MG = 56, S_P4 = 57;

struct Ctx {
  const char *a1, *b1, *a2, *b2, *kv, *a4, *b4r, *b4i, *a5, *b5, *pf4, *pf5, *pfu;
  unsigned lds0;
  int lw, lane;
  bool isY;
};
template <int PH, int NW>
struct Loader {
  static constexpr int KA = PH <= 2 ? OP_ROW : OP_FRAG, KB = PH <= 2 ? OP_TR : PH == 4 ? OP_ROW : OP_TRP;
  static constexpr int LDA = PH == 1 ? 2048 : PH == 2 ? 4096 : PH == 4 ? 65536 : 32768, LDB = PH == 1 ? 6144 : 2048;
  static constexpr int NSUB = PH == 1 ? 3 : 2, SUBOFF = PH == 1 ? 2048 : PH == 4 ? 131072 : 128;
  static constexpr int PA = 16 / NW, PB = 8 / NW;
  unsigned va[2], vb[2];
  __device__ __forceinline__ void init(const Ctx& c) {
#pragma unroll
    for (int j = 0; j < 2; ++j) {
      va[j] = PH == 3 ? c.lane * 16 : piece_off<KA>(c.lw * PA + j, c.lane, LDA);
      vb[j] = piece_off<KB>(c.lw * PB + j, c.lane, LDB);
    }
  }
  __device__ __forceinline__ void stage(const Ctx& c, int t, int s) const {
    const unsigned d = c.lds0 + (s & 3) * SLOT;
    if constexpr (PH == 3) {
#pragma unroll
      for (int h = 0; h < 4 / NW; ++h) {
        const int part = c.lw * (4 / NW) + h;
#pragma unroll
        for (int j = 0; j < 8; ++j)
          blds<1>(mkrs(c.kv), va[0], (part & 1) * (4 << 20) + (part >> 1) * 65536 + t * 8192 + j * 1024, d + part * 8192 + j * 1024);
      }
    } else {
      const char* pa = PH == 1 ? c.a1 : PH == 2 ? c.a2 : PH == 4 ? c.a4 : c.a5;
      const char* pb = PH == 1 ? c.b1 : PH == 2 ? c.b2 : PH == 4 ? (t < 16 ? c.b4r : c.b4i) : c.b5;
      const int sa = t * (KA == OP_ROW ? 128 : 2048), sb = PH == 4 ? (t & 15) * 128 : t * 64 * LDB;
#pragma unroll
      for (int j = 0; j < PA; ++j)
        blds<(PH >= 4 ? APOL : PH == 2 ? DNT : 0)>(mkrs(pa), va[j & 1], sa + (j >> 1) * (KA == OP_ROW ? 16 * LDA : LDA), d + (c.lw * PA + j) * 1024);
#pragma unroll
      for (int u = 0; u < NSUB; ++u)
#pragma unroll
        for (int j = 0; j < PB; ++j)
          blds<(PH == 1 ? DNT : 0)>(mkrs(pb), vb[j & 1], sb + u * SUBOFF + (j >> 1) * 16 * LDB, d + 16384 + u * 8192 + (c.lw * PB + j) * 1024);
    }
  }
};
__device__ __forceinline__ void touch(const void* p, unsigned voff, unsigned soff) {
  asm volatile("buffer_load_dword v255, %0, %1, %2 offen" ::"v"(voff), "s"(mkrs(p)), "s"(__builtin_amdgcn_readfirstlane(soff)) : "memory", "v255");
}

template <bool LD>
__device__ __forceinline__ void attention(const Ctx& c, const bf16x8 (&q)[4], const char* Qw, const char* Kh, const char* Vh, rsrc_t rO,
                                          unsigned ooff, int grp, int wq, const char* pU, gu32* uflags, gu32* myflag, unsigned tag, lptr smem) {
  const int lane = c.lane;
  const bf16x8 qa0 = q[0], qb0 = q[1], qa1 = q[2], qb1 = q[3];
  u32x4 ut;
  lptr usp = smem + wq * SLOT + 32768 + lane * 16;
#define URD(n) do { if constexpr (!LD) ut = *(const LAS u32x4*)(usp + (((t & 1) * 4 + (n)) << 10)); } while (0)
#define UST(n) do { if constexpr (!LD) st_wt(mkrs(pU, t < 2), lane * 16 + ((n) & 1) * 1024 + ((t & 1) * 2 + ((n) >> 1)) * 65536, ut); } while (0)
  Loader<3, 4> l3; Loader<4, 4> l4;
  if constexpr (LD) { l3.init(c); l4.init(c); }
  bf16x8 ones;
  {
    const __bf16 v = (((lane >> 2) ^ (lane >> 4)) & 1) ? (__bf16)0.0f : (__bf16)1.0f;
#pragma unroll
    for (int j = 0; j < 8; ++j) ones[j] = v;
  }
#define RDK(dst, P, n) dst = *(const LAS bf16x8*)((P) + (n) * 1024)
#define RDV(dst, P, n) dst = *(const LAS bf16x8*)((P) + 8192 + (n) * 1024)
  bf16x8 k0a, k0b, k1a, k1b, v0a, v0b, v1a, v1b;
  f32x16 sA, sB, nm0, nm1, o0 = {}, o1 = {};
  f32x4 l0 = {}, l1 = {};
  u32x4 pA0, pA1, pB0 = {}, pB1 = {};
  lptr kb = smem + grp * 16384 + lane * 16;
  int cur = (S_AT & 3) * SLOT;
  RDK(k0a, kb + cur, 0); RDK(k0b, kb + cur, 1); RDK(k1a, kb + cur, 2); RDK(k1b, kb + cur, 3);
  RDV(v0a, kb + cur, 0); RDV(v0b, kb + cur, 1);
  {
    const f32x16 z = {};
    f32x16 s0 = MFMA32(k0a, qa0, z); s0 = MFMA32(k0b, qb0, s0);
    f32x16 s1 = MFMA32(k1a, qa0, z); s1 = MFMA32(k1b, qb0, s1);
    nm0 = splat16(-(__builtin_ceilf(halfmax(__builtin_fmaxf(max16(s0), max16(s1)))) + REF_BIAS));
    s0 = MFMA32(k0a, qa1, z); s0 = MFMA32(k0b, qb1, s0);
    s1 = MFMA32(k1a, qa1, z); s1 = MFMA32(k1b, qb1, s1);
    nm1 = splat16(-(__builtin_ceilf(halfmax(__builtin_fmaxf(max16(s0), max16(s1)))) + REF_BIAS));
  }
  PIN(nm0); PIN(nm1);
  sA = MFMA32(k0a, qa0, nm0); sA = MFMA32(k0b, qb0, sA);
  v1a = v0a; v1b = v0a;
  STAMP(4);
  int t = 0;
  float tl0 = 0.f, tl1 = 0.f, tl2 = 0.f, tl3 = 0.f;
#define NOP (void)0
#define UE(KA, KB_, VA, VB, R0, R1, X2, X3) UNIT(sA, pA0, pA1, sB, KA, KB_, qa1, qb1, nm1, o1, l1, VA, VB, pB0, pB1, R0, R1, X2, X3)
#define UO(KA, KB_, VA, VB, R0, R1, X2, X3) UNIT(sB, pB0, pB1, sA, KA, KB_, qa0, qb0, nm0, o0, l0, VA, VB, pA0, pA1, R0, R1, X2, X3)
#pragma unroll 1
  while (t < 8) {
    bool trig;
#pragma unroll 1
    do {
      lptr cp = kb + cur;
      cur = cur == 3 * SLOT ? 0 : cur + SLOT;
      lptr np = kb + cur;
      SBAR();
      UE(k0a, k0b, v1a, v1b, RDK(k1a, cp, 2), RDK(k1b, cp, 3), URD(0), UST(0));
      UO(k1a, k1b, v0a, v0b, RDV(v1a, cp, 2), RDV(v1b, cp, 3), URD(1), UST(1));
      UE(k1a, k1b, v0a, v0b, RDK(k0a, cp, 4), RDK(k0b, cp, 5), URD(2), UST(2));
      UO(k0a, k0b, v1a, v1b, RDV(v0a, cp, 4), RDV(v0b, cp, 5), URD(3), UST(3));
      UE(k0a, k0b, v1a, v1b, RDK(k1a, cp, 6), RDK(k1b, cp, 7), NOP, NOP);
      UO(k1a, k1b, v0a, v0b, RDV(v1a, cp, 6), RDV(v1b, cp, 7), NOP, NOP);
      if constexpr (LD) {
        drain();
        if (t == 5 && wq == 2 && c.isY) await(uflags, 16, tag, lane, smem);
        bar_l();
        if (t < 4) l3.stage(c, t + 4, S_AT + t + 4);
        else if (t > 4) { if (c.isY) l4.stage(c, t - 5, S_P4 + t - 5); }
        else if (wq == 2) mail(uflags, 16, lane, c.lds0);
        if (t == 2) set_flag(myflag, tag);
        if (t == TP) { touch(c.pf4, lane * 128, wq * 8192); touch(c.pf5, lane * 128, wq * 8192); }
        if (t == TU && c.isY) {
#pragma unroll
          for (int k = 0; k < 4; ++k) touch(c.pfu, lane * 128, wq * 32768 + k * 8192);
        }
      } else {
        bar_vl<4>();
      }
      SBAR();
      UE(k1a, k1b, v0a, v0b, RDK(k0a, np, 0), RDK(k0b, np, 1), NOP, NOP);
      UO(k0a, k0b, v1a, v1b, RDV(v0a, np, 0), RDV(v0b, np, 1); trig = __any(l0[0] + l1[0] > L_LIMIT), NOP, NOP);
      ++t;
    } while (t < 8 && !trig);
    if (t >= 8) break;
    asm volatile("s_nop 7" : "+v"(l0), "+v"(l1));
    pB1[2] = pk(tl0, tl1); pB1[3] = pk(tl2, tl3);
    o1 = MFMA32(v1a, BC(pB0), o1); o1 = MFMA32(v1b, BC(pB1), o1);
    l1 = MFMA16(ones, BC(pB0), l1); l1 = MFMA16(ones, BC(pB1), l1);
    pB0 = u32x4{}; pB1 = u32x4{}; tl0 = 0.f; tl1 = 0.f; tl2 = 0.f; tl3 = 0.f;
    float f0, f1;
    RECENTRE(nm0, o0, l0, f0);
    RECENTRE(nm1, o1, l1, f1);
#pragma unroll
    for (int r = 0; r < 16; ++r) sA[r] -= f0;
    asm volatile("s_nop 1" : "+v"(l0), "+v"(l1), "+v"(pB0), "+v"(pB1));
  }
  STAMP(5);
  asm volatile("s_nop 7" : "+v"(l0), "+v"(l1));
  pB1[2] = pk(tl0, tl1); pB1[3] = pk(tl2, tl3);
  o1 = MFMA32(v1a, BC(pB0), o1); o1 = MFMA32(v1b, BC(pB1), o1);
  l1 = MFMA16(ones, BC(pB0), l1); l1 = MFMA16(ones, BC(pB1), l1);
  lptr mg = smem + (S_MG & 3) * SLOT + lane * 16;
  {
    const f32x16& og = LD ? o0 : o1;
    lptr d = mg + ((1 - grp) * 4 + wq) * 5120;
#pragma unroll
    for (int q = 0; q < 4; ++q) *(LAS f32x4*)(d + q * 1024) = f32x4{og[4 * q], og[4 * q + 1], og[4 * q + 2], og[4 * q + 3]};
    *(LAS f32x4*)(d + 4096) = f32x4{LD ? l0[0] : l1[0], LD ? nm0[0] : nm1[0], 0.f, 0.f};
  }
  bar_l();
  f32x16 o = LD ? o1 : o0;
  float l = LD ? l1[0] : l0[0];
  {
    lptr d = mg + (grp * 4 + wq) * 5120;
    const f32x4 e = *(const LAS f32x4*)(d + 4096);
    const float nmk = LD ? nm1[0] : nm0[0], nmin = __builtin_fminf(nmk, e[1]);
    const int ek = (int)__builtin_fmaxf(nmin - nmk, -400.f), eo = (int)__builtin_fmaxf(nmin - e[1], -400.f);
    l = __builtin_amdgcn_ldexpf(l, ek) + __builtin_amdgcn_ldexpf(e[0], eo);
#pragma unroll
    for (int q = 0; q < 4; ++q) {
      const f32x4 x = *(const LAS f32x4*)(d + q * 1024);
#pragma unroll
      for (int r = 0; r < 4; ++r) o[4 * q + r] = __builtin_amdgcn_ldexpf(o[4 * q + r], ek) + __builtin_amdgcn_ldexpf(x[r], eo);
    }
  }
  float chk = l;
#pragma unroll
  for (int r = 0; r < 16; ++r) chk += __builtin_fabsf(o[r]);
  u32x4 x, y;
  if (__builtin_expect(__any(!(chk < 3.0e38f) || !(l > 0.f)), 0)) attn_safe(Kh, Vh, Qw + grp * 2048, lane, x, y);
  else pack16(o, 1.0f / l, x, y);
  const unsigned vo = ooff + grp * 65536 + ((lane >> 4) & 1) * 32768 + (lane >> 5) * 256 + (lane & 15) * 16;
  st_wt(rO, vo, x); st_wt(rO, vo + 512, y);
}

__device__ __forceinline__ void store_out(const f32x4 (&acc)[4][4], u16* out, int row0, int col0, int lane) {
#pragma unroll
  for (int mi = 0; mi < 4; ++mi) {
    const u32x4 c0 = natural(pack2(acc[mi][0], acc[mi][1], 1.0f)), c1 = natural(pack2(acc[mi][2], acc[mi][3], 1.0f));
    if (FULL) {
      u32x4 d0, d1;
#pragma unroll
      for (int r = 0; r < 4; ++r) {
        d0[r] = __builtin_amdgcn_update_dpp(c0[r], c1[r], 0x128, 0xf, 0xc, false);
        d1[r] = __builtin_amdgcn_update_dpp(c1[r], c0[r], 0x128, 0xf, 0x3, false);
      }
      u16* p = out + (row0 + mi * 16 + (lane & 7)) * 2048 + col0 + ((lane >> 3) & 1) * 32 + (lane >> 4) * 8;
      *(u32x4*)p = d0; *(u32x4*)(p + 8 * 2048) = d1;
    } else {
      u16* p = out + (row0 + mi * 16 + (lane & 15)) * 2048 + col0 + (lane >> 4) * 8;
      *(u32x4*)p = c0; *(u32x4*)(p + 32) = c1;
    }
  }
}

__global__ __launch_bounds__(512, 2) void fused(const u16* __restrict__ xA, const u16* __restrict__ Wsr, const u16* __restrict__ Wsi,
                                                const u16* __restrict__ Wdr, const u16* __restrict__ Wdi, const u16* __restrict__ xB,
                                                const u16* __restrict__ wqkv, const u16* __restrict__ wo, u16* __restrict__ out,
                                                char* ws) {
  extern __shared__ __attribute__((aligned(16))) char smem_[];
  lptr smem = (lptr)smem_;
  const char* pc;
  asm volatile("s_getpc_b64 %0" : "=s"(pc));
  const int lane = threadIdx.x & 63, w = __builtin_amdgcn_readfirstlane(threadIdx.x >> 6), wm = (w >> 1) & 1, wn = w & 1;
  const int xcd = blockIdx.x & 7, i = blockIdx.x >> 3;
  gu32* flags = (gu32*)ws + 64;
  char* QKV = ws + (1 << 20);
  char* Of = QKV + (12 << 20);
  char* Uf = Of + (4 << 20);
  const int j2 = xcd * 2 + (i >> 4), tb1 = i & 15;
  const int tbA = xcd * 2 + (i >> 4), im = (i >> 3) & 1, nb2 = i & 7;
  const int head = xcd * 4 + (i >> 3), qb = i & 7;
  const bool isY = i < 16;
  const int tb5 = (xcd >> 1) * 4 + (i & 3), nb5 = (xcd & 1) * 4 + ((i >> 2) & 3);
  const char* Kh = QKV + (4 << 20) + head * 131072;
  const char* Vh = QKV + (8 << 20) + head * 131072;
  const int qt = qb * 8 + (w & 3) * 2;
  const char* Qw = QKV + (head * 64 + qt) * 2048;
  const unsigned ooff = qt * 65536 + head * 1024;
  Ctx c;
  c.lane = lane; c.lw = w; c.lds0 = (unsigned)(unsigned long)smem;
  c.a1 = (const char*)(xB + tb1 * 128 * D_); c.b1 = (const char*)(wqkv + j2 * 64);
  ST3(64, 109);
  {
    Loader<1, 8> l0; l0.init(c);
    l0.stage(c, 0, 0);
  }
  const void* p1 = w == 7 ? (const void*)c.b1 : (const void*)(xB + blockIdx.x * 8 * D_);
  const unsigned vo1 = w == 7 ? (lane < 48 ? (tb1 * 16 + (lane & 15)) * 6144 + (lane >> 4) * 2048 : 0u) : ((lane >> 2) & 7) * 2048 + (lane & 3) * 128;
  const unsigned qs1 = w == 7 ? 256 * 6144 : 512;
  if (w >= 6) {
#pragma unroll
    for (int k = 0; k < NQE; ++k) touch(p1, vo1, k * qs1);
  }
  SBAR();
  ST3(64, 112); ST3(256, 113); ST3(384, 114);
  c.lw = w & 3;
  STAMP(0);
  if (__builtin_expect(w >= 4, LK)) {
    c.a2 = (const char*)((im ? Wsi : Wsr) + tbA * 128 * S_); c.b2 = (const char*)(xA + nb2 * 128);
    c.kv = Kh;
    c.a4 = Uf + tb5 * 8 * 65536; c.b4r = (const char*)(Wdr + nb5 * 128 * D_); c.b4i = (const char*)(Wdi + nb5 * 128 * D_);
    c.pf4 = (const char*)Wdr + ((xcd & 1) * 32 + i) * 32768; c.pf5 = (const char*)Wdi + ((xcd & 1) * 32 + i) * 32768;
    c.pfu = c.a4 + (nb5 & 3) * 131072;
    c.a5 = Of + tb5 * 8 * 32768; c.b5 = (const char*)(wo + nb5 * 128);
    c.isY = isY;
    gu32 *fq = flags + (head >> 1) * 16, *fo = flags + 512 + (tb5 >> 1) * 32;
    unsigned tag = 0;
    bf16x8 q[4];
    auto load_q = [&]() {
#pragma unroll
      for (int k = 0; k < 4; ++k) blds<1>(mkrs(Qw), lane * 16, k * 1024, c.lds0 + c.lw * SLOT + 32768 + k * 1024);
    };
    auto read_q = [&]() {
#pragma unroll
      for (int k = 0; k < 4; ++k) q[k] = *(const LAS bf16x8*)(smem + c.lw * SLOT + 32768 + k * 1024 + lane * 16);
    };
    if (__builtin_expect(w < 6, 1)) {
      {
        ST3(256, 110);
        Loader<1, 2> l1; l1.init(c);
        l1.stage(c, 1, 1);
        ST3(256, 111);
#pragma unroll 1
        for (int s = -1; s <= CUP; ++s) { bar_v<20>(); l1.stage(c, s + 3, s + 3); }
        l1.stage(c, CUP + 4, CUP + 4);
#pragma unroll 1
        for (int s = CUP + 1; s < 12; ++s) { bar_v<40>(); l1.stage(c, s + 4, s + 4); }
      }
      {
        Loader<2, 2> l2; l2.init(c);
#pragma unroll 1
        for (int s = 12; s < 44; ++s) { bar_v<32>(); l2.stage(c, s - 12, s + 4); }
      }
      Loader<3, 2> l3; l3.init(c);
      bar_v<32>();
      load_q();
      l3.stage(c, 0, 48);
#pragma unroll 1
      for (int s = 45; s < 47; ++s) { bar_v<32>(); l3.stage(c, s - 44, s + 4); }
      asm volatile("s_waitcnt vmcnt(32)" ::: "memory");
      read_q();
      bar_l();
      l3.stage(c, 3, 51);
    } else {
      const bool u = w == 7;
      const int b = blockIdx.x;
      ST3(384, 115);
      const unsigned vo2 = (lane >> 2) * 4096 + (lane & 3) * 128;
      const char* pKVo = QKV + (4 << 20) + ((j2 * 2) * 64 + tb1 * 4 + (w & 1) * 2) * 2048;
      bar_v<NQE>();
#pragma unroll 1
      for (int s = 0; s < 44; ++s) {
        if (s == FL) drain();
        bar_l();
        if (s == 15 || s == 29 || s == 43) ST4(s);
#pragma unroll
        for (int k = NQE; k < 4; ++k)
          if (s == QS + (k - NQE) * QI) touch(p1, vo1, k * qs1);
        if (s == CS) {
          if (u) {
#pragma unroll
            for (int k = 0; k < CODEPF; ++k) touch(pc, lane * 128, k * 8192);
          } else touch(ws, 0, 0);
        }
        if (s == 12) {
          asm volatile("s_load_dword %0, %1, 0x0\n\ts_waitcnt lgkmcnt(0)" : "=s"(tag) : "s"(ws) : "memory");
          tag = tag * 1664525u + 1013904223u;
        }
        if (s == PWS + PWI * nb2 && !isY) {
#pragma unroll
          for (int k = 0; k < 2; ++k) touch(xA + (nb2 * 256 + (xcd * 2 + im) * 16 + (w & 1) * 8) * D_, lane * 128, k * 8192);
#pragma unroll
          for (int k = 0; k < 8; ++k) touch(c.a2 + nb2 * 512 - (w & 1) * 128 * 4096, vo2, k * 65536);
        }
        if (s >= 16 && s < 16 + KVN) {
#pragma unroll
          for (int m = 0; m < 16 / KVN; ++m) {
            const int ix = (s - 16) * (16 / KVN) + m, n = ix >> 1, h = ix & 1;
            const u32x4 v = *(const LAS u32x4*)(smem + ((w & 1) * 2 + h) * SLOT + 32768 + n * 1024 + lane * 16);
            __builtin_amdgcn_raw_buffer_store_b128(v, mkrs(pKVo), lane * 16, h * 131072 + (n >> 2) * (4 << 20) + (n & 3) * 1024, 16);
          }
        }
        if (s == FL) set_flag(flags + j2 * 16 + tb1, tag);
        if (s == 40 && !u) mail(fq, 16, lane, c.lds0);
      }
      drain();
      if (!u) await(fq, 16, tag, lane, smem);
      bar_l();
      load_q();
      bar_l(); bar_l();
      drain();
      read_q();
      bar_l();
      touch(u ? Wdi : Wdr, lane * 128, b * 8192);
      touch(wo, (lane & 31) * 128, b * 8192 + (u ? 4096 : 0));
    }
    STAMP(3);
    attention<true>(c, q, Qw, Kh, Vh, mkrs(Of), ooff, 1, w & 3, nullptr, flags + 1024 + tb5 * 16, flags + 1024 + tbA * 16 + (i & 15), tag, smem);
    STAMP(6);
    if (!isY) {
      bar_vl<0>();
      if (w < 6) {
        Loader<5, 2> l5; l5.init(c);
        bar_l();
        l5.stage(c, 0, S_P4); l5.stage(c, 1, S_P4 + 1);
        bar_v<16>();
        l5.stage(c, 2, S_P4 + 2);
#pragma unroll 1
        for (int t = 3; t < 16; ++t) { l5.stage(c, t, S_P4 + t); bar_v<32>(); }
        bar_v<16>(); bar_v<0>(); bar_v<0>();
      } else {
        set_flag(flags + 512 + qb * 32 + head, tag);
        if (w == 6) { mail(fo, 32, lane, c.lds0); drain(); await(fo, 32, tag, lane, smem); }
        bar_l();
      }
      return;
    }
    bar_vl<2>();
    if (w < 6) {
      Loader<4, 2> l4; l4.init(c);
      l4.stage(c, 3, S_P4 + 3);
#pragma unroll 1
      for (int t = 4; t < 6; ++t) { bar_v<34>(); l4.stage(c, t, S_P4 + t); }
#pragma unroll 1
      for (int t = 6; t < 32; ++t) { bar_v<32>(); l4.stage(c, t, S_P4 + t); }
      bar_v<32>(); bar_v<16>(); bar_v<0>(); bar_v<0>();
    } else {
#pragma unroll 1
      for (int s = S_P4; s < S_P4 + 17; ++s) {
        if (s == S_P4 + OFL) drain();
        bar_l();
        if (s == S_P4 + OFL) set_flag(flags + 512 + qb * 32 + head, tag);
        if (blockIdx.x == 0 && w == 6) {
          if (s == S_P4 + 8) blds<1>(mkrs((const void*)flags), lane * 16, 0, c.lds0 + MAIL + 1024);
          if (s == S_P4 + 16) {
            drain();
            u32x4 f = *(volatile LAS u32x4*)(smem + MAIL + 1024 + lane * 16);
#pragma unroll 1
            for (unsigned spins = 0; spins < 400000u && !__all(f[0] == tag && f[1] == tag && f[2] == tag && f[3] == tag); ++spins) {
              __builtin_amdgcn_s_sleep(2);
              f = __builtin_amdgcn_raw_buffer_load_b128(mkrs((const void*)flags), lane * 16, 0, 16);
            }
            set_flag((gu32*)ws, tag);
          }
        }
      }
    }
    return;
  }
  const unsigned lds0 = c.lds0;
  lptr park = smem + w * SLOT + 32768;
  ST3(64, 116);
  ST3(64, 117);
  bar_v<0>();
  ST3(64, 118);
  {
    FragRd<OP_ROW, 1> ra; ra.init(wm * 4, lane, lds0 + (S_P1 & 3) * SLOT);
    FragRd<OP_TR, 2> rb; rb.init(wn * 2, lane, lds0 + (S_P1 & 3) * SLOT + 16384);
    f32x4 acc[4][6] = {};
    consume<4, 6, 4, S_P1, 16, 0>(acc, ra, rb);
    STAMP(1);
    const rsrc_t rQ = mkrs(QKV + ((j2 * 2 + wn) * 64 + tb1 * 4 + wm * 2) * 2048);
    const int cc = lane & 15, g = lane >> 4;
#pragma unroll
    for (int mi = 0; mi < 4; ++mi) {
      const unsigned blk = (mi >> 1) * 2048 + g * 512;
      st_wt(rQ, blk + ((mi & 1) * 16 + cc) * 16, pack2(acc[mi][0], acc[mi][1], 0.17677669529663687f * 1.4426950408889634f));
      *(LAS u32x4*)(park + blk + ((cc >> 3) * 16 + (mi & 1) * 8 + (cc & 7)) * 16) = pack2(acc[mi][2], acc[mi][3], 1.0f);
      *(LAS u32x4*)(park + 4096 + blk + ((mi & 1) * 16 + cc) * 16) = pack2(acc[mi & 2][4 + (mi & 1)], acc[(mi & 2) + 1][4 + (mi & 1)], 1.0f);
    }
    STAMP(2);
  }
  bf16x8 q[4];
  {
    FragRd<OP_ROW, 1> ra; ra.init(wm * 4, lane, lds0 + (S_P2 & 3) * SLOT);
    FragRd<OP_TR, 4> rb; rb.init(0, lane, lds0 + (S_P2 & 3) * SLOT + 16384 + wn * 8192);
    f32x4 acc[4][4] = {};
    consume<4, 4, 4, S_P2, 32, 11>(acc, ra, rb);
    STAMP(3);
#pragma unroll
    for (int k = 0; k < 4; ++k) q[k] = *(const LAS bf16x8*)(park + k * 1024 + lane * 16);
    const float sc = im ? -1.0f : 1.0f;
#pragma unroll
    for (int mi = 0; mi < 4; ++mi)
#pragma unroll
      for (int n2 = 0; n2 < 2; ++n2)
        *(LAS u32x4*)(park + (mi * 2 + n2) * 1024 + lane * 16) = natural(pack2(acc[mi][2 * n2], acc[mi][2 * n2 + 1], sc));
  }
  attention<false>(c, q, Qw, Kh, Vh, mkrs(Of), ooff, 0, w, Uf + (tbA * 8 + wm * 4) * 65536 + (im * 32 + nb2 * 4 + wn * 2) * 1024,
                   nullptr, nullptr, 0u, smem);
  STAMP(6);
  FragRd<OP_FRAG, 1> ra; ra.init(wm * 4, lane, lds0 + (S_P4 & 3) * SLOT);
  f32x4 acc[4][4] = {};
  if (isY) {
    FragRd<OP_ROW, 1> rb; rb.init(wn * 4, lane, lds0 + (S_P4 & 3) * SLOT + 16384);
    bar_l();
    consume<4, 4, 4, S_P4, 32, OFL>(acc, ra, rb);
  } else {
    FragRd<OP_TRP, 4> rb; rb.init(0, lane, lds0 + (S_P4 & 3) * SLOT + 16384 + wn * 8192);
    bar_vl<0>(); bar_l(); bar_l();
    consume<4, 4, 4, S_P4, 16, 0>(acc, ra, rb);
  }
  STAMP(7);
  STAMP(8);
  store_out(acc, out, tb5 * 128 + wm * 64, (isY ? 0 : 1024) + nb5 * 128 + wn * 64, lane);
}

extern "C" void kernel_launch(void* const* d_in, const int* in_sizes, int n_in, void* d_out, int out_size, void* d_ws,
                              size_t ws_size, hipStream_t stream) {
  (void)hipFuncSetAttribute((const void*)fused, hipFuncAttributeMaxDynamicSharedMemorySize, LDS_BYTES);
  fused<<<256, 512, LDS_BYTES, stream>>>((const u16*)d_in[0], (const u16*)d_in[1], (const u16*)d_in[2], (const u16*)d_in[3],
                                         (const u16*)d_in[4], (const u16*)d_in[5], (const u16*)d_in[6], (const u16*)d_in[7],
                                         (u16*)d_out, (char*)d_ws);
}
```

```cpp
#include <hip/hip_runtime.h>
#include <hip/hip_bf16.h>
#include <type_traits>

typedef unsigned short u16;
typedef __attribute__((ext_vector_type(8))) __bf16 bf16x8;
typedef __attribute__((ext_vector_type(2))) __bf16 bf16x2;
typedef __attribute__((ext_vector_type(2))) float f32x2;
typedef __attribute__((ext_vector_type(4))) float f32x4;
typedef __attribute__((ext_vector_type(16))) float f32x16;
typedef __attribute__((ext_vector_type(4))) unsigned u32x4;
typedef __attribute__((ext_vector_type(2))) unsigned u32x2;
typedef __attribute__((ext_vector_type(4))) short s16x4;
#define LAS __attribute__((address_space(3)))
#define GAS __attribute__((address_space(1)))
typedef LAS char* lptr;
typedef GAS unsigned gu32;
typedef __amdgpu_buffer_rsrc_t rsrc_t;

#define SBAR() __builtin_amdgcn_sched_barrier(0)
#define PIN(x) asm volatile("" : "+v"(x))
#define MFMA32(a, b, c) __builtin_amdgcn_mfma_f32_32x32x16_bf16((a), (b), (c), 0, 0, 0)
#define MFMA16(a, b, c) __builtin_amdgcn_mfma_f32_16x16x32_bf16((a), (b), (c), 0, 0, 0)
#define BC(x) __builtin_bit_cast(bf16x8, x)
#define RLX_AGENT __ATOMIC_RELAXED, __HIP_MEMORY_SCOPE_AGENT
template <int N> __device__ __forceinline__ void bar_vl() { asm volatile("s_waitcnt vmcnt(%0) lgkmcnt(0)\n\ts_barrier" ::"n"(N) : "memory"); }

constexpr int CUP = 0, NQE = 1, QS = 0, QO = 2, CS = 2, PWS = 1, PWI = 4, KVN = 4, FL = 27, TP = 4, TU = 5, UW = 6, OFL = 2;
constexpr int S_ = 2048, D_ = 1024, SLOT = 40960, LDS_BYTES = 4 * SLOT;
enum { OP_ROW = 0, OP_FRAG = 1, OP_TR = 2, OP_TRP = 3 };

__device__ __forceinline__ unsigned pk(float lo, float hi) {
  f32x2 v = {lo, hi};
  return __builtin_bit_cast(unsigned, __builtin_convertvector(v, bf16x2));
}
__device__ __forceinline__ rsrc_t mkrs(const void* p, bool on = true) {
  return __builtin_amdgcn_make_buffer_rsrc((void*)p, 0, on ? 0x7fffffff : 0, 0x00020000);
}
template <int POL = 0>
__device__ __forceinline__ void blds(rsrc_t r, unsigned voff, unsigned soff, unsigned l) {
  if constexpr (POL == 1) asm volatile("s_mov_b32 m0, %0\n\ts_nop 0\n\tbuffer_load_dwordx4 %1, %2, %3 offen sc1 lds" ::"s"(l), "v"(voff), "s"(r), "s"(soff) : "memory");
  else asm volatile("s_mov_b32 m0, %0\n\ts_nop 0\n\tbuffer_load_dwordx4 %1, %2, %3 offen lds" ::"s"(l), "v"(voff), "s"(r), "s"(soff) : "memory");
}
__device__ __forceinline__ bf16x8 ld_dev(rsrc_t r, unsigned voff, unsigned soff = 0) {
  return __builtin_bit_cast(bf16x8, __builtin_amdgcn_raw_buffer_load_b128(r, voff, soff, 16));
}
__device__ __forceinline__ void st_wt(rsrc_t r, unsigned voff, u32x4 v) { __builtin_amdgcn_raw_buffer_store_b128(v, r, voff, 0, 16); }

template <int KIND>
__device__ __forceinline__ unsigned piece_off(int p, int i, int ld) {
  if constexpr (KIND == OP_ROW) return (8 * p + (i >> 3)) * ld + (((i & 7) ^ (((p & 1) << 2) | (i >> 4))) << 4);
  else if constexpr (KIND == OP_FRAG) return (p >> 1) * ld + (p & 1) * 1024 + i * 16;
  else if constexpr (KIND == OP_TR) return (8 * p + (i >> 3)) * ld + (((i & 7) ^ ((((i >> 4) & 1) | ((p & 1) << 1)) << 1)) << 4);
  else return (8 * p + (i >> 3)) * ld + (((i & 7) ^ ((i >> 4) << 1)) << 4);
}
template <int KIND, int NBASE>
struct FragRd {
  static constexpr int NB = KIND == OP_ROW ? 2 : (KIND == OP_FRAG ? 1 : NBASE);
  static constexpr int OPS = KIND >= OP_TR ? 2 : 1;
  unsigned base[NB];
  __device__ __forceinline__ void init(int b0, int lane, unsigned img) {
    const int r = lane & 15, g = lane >> 4;
    if constexpr (KIND == OP_ROW) {
#pragma unroll
      for (int ks = 0; ks < 2; ++ks) base[ks] = img + (b0 * 16 + r) * 128 + (((4 * ks + g) ^ ((r >> 1) & 7)) << 4);
    } else if constexpr (KIND == OP_FRAG) {
      base[0] = img + b0 * 2048 + lane * 16;
    } else {
      const int q = r >> 2, p = lane & 3, f = ((q >> 1) & 1) | ((g & 1) << 1);
      const int row = (KIND == OP_TR ? 8 * g : 16 * (g >> 1) + 4 * (g & 1)) + q;
#pragma unroll
      for (int b = 0; b < NBASE; ++b) base[b] = img + row * 128 + (((((b0 + b) ^ f) << 1) | (p >> 1)) << 4) + (p & 1) * 8;
    }
  }
  __device__ __forceinline__ void step(int k, int d) { base[k] += d; PIN(base[k]); }
  __device__ __forceinline__ void rd(u32x4& f, int b, int ks, int half) const {
    if constexpr (KIND == OP_ROW) f = *(const LAS u32x4*)(unsigned long)(base[ks] + b * 2048);
    else if constexpr (KIND == OP_FRAG) f = *(const LAS u32x4*)(unsigned long)(base[0] + b * 2048 + ks * 1024);
    else {
      const u32x2 x = __builtin_bit_cast(u32x2, __builtin_amdgcn_ds_read_tr16_b64_v4i16(
          (LAS s16x4*)(unsigned long)(base[b % NBASE] + (b / NBASE) * 8192 + ks * 4096 + half * (KIND == OP_TR ? 512 : 1024))));
      f[2 * half] = x[0]; f[2 * half + 1] = x[1];
    }
  }
};

__device__ __forceinline__ u32x4 pack2(const f32x4& x, const f32x4& y, float sc) {
  return u32x4{pk(x[0] * sc, x[1] * sc), pk(x[2] * sc, x[3] * sc), pk(y[0] * sc, y[1] * sc), pk(y[2] * sc, y[3] * sc)};
}
__device__ __forceinline__ u32x4 natural(u32x4 c) {
  auto x = __builtin_amdgcn_permlane32_swap(c[0], c[2], false, false);
  auto y = __builtin_amdgcn_permlane32_swap(c[1], c[3], false, false);
  auto p = __builtin_amdgcn_permlane16_swap(x[0], x[1], false, false);
  auto q = __builtin_amdgcn_permlane16_swap(y[0], y[1], false, false);
  return u32x4{p[0], q[0], p[1], q[1]};
}


__device__ __forceinline__ void bar_l() { asm volatile("s_waitcnt lgkmcnt(0)\n\ts_barrier" ::: "memory"); }
template <int N> __device__ __forceinline__ void bar_v() { asm volatile("s_waitcnt vmcnt(%0)\n\ts_barrier" ::"n"(N) : "memory"); }
__device__ __forceinline__ void drain() { asm volatile("s_waitcnt vmcnt(0)" ::: "memory"); }
__device__ __forceinline__ void set_flag(gu32* flag, unsigned tag) {
  if (threadIdx.x == 384) __hip_atomic_store(flag, tag, RLX_AGENT);
}
constexpr int MAIL = 32768 + 4096;
__device__ __forceinline__ void mail(gu32* flags, int ix, unsigned lds0) {
  asm volatile("s_mov_b32 m0, %0\n\ts_nop 0\n\tbuffer_load_dword %1, %2, 0 offen sc1 lds"
               ::"s"(lds0 + MAIL), "v"(ix * 4), "s"(mkrs((const void*)flags)) : "memory");
}
__device__ __forceinline__ void await(gu32* flags, int ix, unsigned tag, int lane, lptr smem) {
  if (__builtin_expect(!__all(*(volatile LAS unsigned*)(smem + MAIL + lane * 4) == tag), 0)) {
#pragma unroll 1
    for (unsigned spins = 0; spins < 400000u; ++spins) {
      if (__all(__hip_atomic_load(flags + ix, RLX_AGENT) == tag)) break;
      __builtin_amdgcn_s_sleep(2);
    }
  }
  __builtin_amdgcn_fence(__ATOMIC_ACQUIRE, "wavefront");
}
#define WAIT_LGKM(n) __builtin_amdgcn_s_waitcnt(0xC07F | ((n) << 8))

template <int MI, int NI, int NSWAP, int S0, int NT, int ND, class RA, class RB>
__device__ __forceinline__ void consume(f32x4 (&acc)[MI][NI], RA& ra, RB& rb) {
  u32x4 a0[MI], b0[NI], a1[MI], b1[NI];
  constexpr int NM = MI * NI, NBO = NI * RB::OPS, NR = MI + NBO, NU = RA::NB + RB::NB;
  auto rdop = [&](u32x4(&na)[MI], u32x4(&nb)[NI], int ks, int z) {
    if (z == 0) ra.rd(na[0], 0, ks, 0);
    else if (z <= NBO) rb.rd(nb[(z - 1) / RB::OPS], (z - 1) / RB::OPS, ks, (z - 1) % RB::OPS);
    else ra.rd(na[z - NBO], z - NBO, ks, 0);
  };
  auto mma = [&](const u32x4(&fa)[MI], const u32x4(&fb)[NI], int i) {
    const int mi = i / NI, ni = i % NI;
    acc[mi][ni] = ni < NSWAP ? MFMA16(BC(fb[ni]), BC(fa[mi]), acc[mi][ni]) : MFMA16(BC(fa[mi]), BC(fb[ni]), acc[mi][ni]);
    SBAR();
  };
#pragma unroll
  for (int z = 0; z < NR; ++z) { rdop(a0, b0, 0, z); SBAR(); }
  int slot = S0 & 3;
#pragma unroll 1
  for (int seg = ND ? 0 : 1; seg < 2; ++seg) {
#pragma unroll 1
    for (int t = seg ? NT - ND : ND; t > 0; --t) {
      slot = (slot + 1) & 3;
      const int d = slot == 0 ? -3 * SLOT : SLOT;
      WAIT_LGKM(MI - 1);
      SBAR();
#pragma unroll
      for (int i = 0; i < NM + 2; ++i) {
        if (i < NM) mma(a0, b0, i);
        else { if (i == NM) { WAIT_LGKM(0); SBAR(); } mma(a1, b1, i - NM); }
        if (i < NR) rdop(a1, b1, 1, i);
        else {
          constexpr int G = NM + 2 - NR;
#pragma unroll
          for (int u = (i - NR) * NU / G; u < (i - NR + 1) * NU / G; ++u) { if (u < RA::NB) ra.step(u, d); else rb.step(u - RA::NB, d); }
        }
        SBAR();
      }
      asm volatile("s_barrier" ::: "memory");
      SBAR();
      constexpr int G1 = NM - 2;
#pragma unroll
      for (int i = 0; i < G1; ++i) {
        if (NR > G1 && i < NR - G1) { rdop(a0, b0, 0, 2 * i); SBAR(); }
        mma(a1, b1, i + 2);
        const int z = NR > G1 ? (i < NR - G1 ? 2 * i + 1 : i + NR - G1) : i;
        if (z < NR) rdop(a0, b0, 0, z);
        SBAR();
      }
    }
    if (ND) drain();
  }
}

__device__ __forceinline__ void pack16(const f32x16& a, float sc, u32x4& c0, u32x4& c1) {
  c0 = u32x4{pk(a[0] * sc, a[1] * sc), pk(a[2] * sc, a[3] * sc), pk(a[4] * sc, a[5] * sc), pk(a[6] * sc, a[7] * sc)};
  c1 = u32x4{pk(a[8] * sc, a[9] * sc), pk(a[10] * sc, a[11] * sc), pk(a[12] * sc, a[13] * sc), pk(a[14] * sc, a[15] * sc)};
}
__device__ __forceinline__ float halfmax(float m) {
  auto rr = __builtin_amdgcn_permlane32_swap(__float_as_uint(m), __float_as_uint(m), false, false);
  return __builtin_fmaxf(__uint_as_float(rr[0]), __uint_as_float(rr[1]));
}
__device__ __forceinline__ float max16(const f32x16& s) {
  float a = __builtin_fmaxf(s[0], s[1]);
#pragma unroll
  for (int r = 2; r < 16; ++r) a = __builtin_fmaxf(a, s[r]);
  return a;
}
__device__ __forceinline__ f32x16 splat16(float v) {
  f32x16 r;
#pragma unroll
  for (int i = 0; i < 16; ++i) r[i] = v;
  return r;
}
constexpr float REF_BIAS = 96.0f;
constexpr float L_LIMIT = 1.099511627776e12f;

__device__ __forceinline__ void attn_safe(const char* Kh, const char* Vh, const char* Qt, int lane, u32x4& c0, u32x4& c1) {
  const rsrc_t rq = mkrs(Qt), rk = mkrs(Kh), rv = mkrs(Vh);
  const bf16x8 q0 = ld_dev(rq, lane * 16), q1 = ld_dev(rq, 1024 + lane * 16);
  float m = -3.0e38f, l = 0.f;
  f32x16 o = {};
  const f32x16 z = {};
#pragma unroll 1
  for (int kt = 0; kt < 64; ++kt) {
    const bf16x8 k0 = ld_dev(rk, lane * 16, kt * 2048), k1 = ld_dev(rk, 1024 + lane * 16, kt * 2048);
    const bf16x8 v0 = ld_dev(rv, lane * 16, kt * 2048), v1 = ld_dev(rv, 1024 + lane * 16, kt * 2048);
    f32x16 s = MFMA32(k0, q0, z);
    s = MFMA32(k1, q1, s);
    const float mn = __builtin_fmaxf(m, halfmax(max16(s)));
    const float al = __builtin_amdgcn_exp2f(m - mn);
    m = mn;
    float rs = 0.f;
#pragma unroll
    for (int r = 0; r < 16; ++r) { s[r] = __builtin_amdgcn_exp2f(s[r] - mn); o[r] *= al; }
    u32x4 p0, p1;
    pack16(s, 1.0f, p0, p1);
#pragma unroll
    for (int r = 0; r < 4; ++r) {
      rs += __uint_as_float(p0[r] << 16) + __uint_as_float(p0[r] & 0xffff0000u);
      rs += __uint_as_float(p1[r] << 16) + __uint_as_float(p1[r] & 0xffff0000u);
    }
    l = l * al + rs;
    o = MFMA32(v0, BC(p0), o);
    o = MFMA32(v1, BC(p1), o);
  }
  auto rr = __builtin_amdgcn_permlane32_swap(__float_as_uint(l), __float_as_uint(l), false, false);
  l = __uint_as_float(rr[0]) + __uint_as_float(rr[1]);
  pack16(o, 1.0f / l, c0, c1);
}

#define EXP2(x) __builtin_amdgcn_exp2f(x)
#define X1(S, i) do { S[i] = EXP2(S[i]); PIN(S[i]); SBAR(); } while (0)
#define XT(T, S, i) do { T = EXP2(S[i]); PIN(T); SBAR(); } while (0)
#define CV(P, j, S, i) do { P[j] = pk(S[i], S[i + 1]); PIN(P[j]); SBAR(); } while (0)
#define CT(P, j, A, B) do { P[j] = pk(A, B); PIN(P[j]); SBAR(); } while (0)
#define SUMP(L, P) asm volatile("v_mfma_f32_16x16x32_bf16 %0, %1, %2, %0" : "+v"(L) : "v"(ones), "v"(P))
#define UNIT(S, Pn0, Pn1, Sn, KA, KB_, Qa, Qb, NM, O, L, VA, VB, Pp0, Pp1, RD0, RD1, X2, X3)     \
  do {                                                                                          \
    Sn = MFMA32(KA, Qa, NM); SBAR();                                                            \
    X1(S, 0); RD0; SBAR(); CT(Pp1, 2, tl0, tl1); X1(S, 1); X1(S, 2);                            \
    Sn = MFMA32(KB_, Qb, Sn); SBAR();                                                           \
    X1(S, 3); RD1; SBAR(); CT(Pp1, 3, tl2, tl3); X1(S, 4); X1(S, 5);                            \
    O = MFMA32(VA, BC(Pp0), O); SBAR();                                                         \
    CV(Pn0, 0, S, 0); X1(S, 6); X2; SBAR(); X1(S, 7); CV(Pn0, 1, S, 2);                         \
    SUMP(L, Pp0); SBAR();                                                                       \
    X1(S, 8); X1(S, 9); CV(Pn0, 2, S, 4);                                                       \
    O = MFMA32(VB, BC(Pp1), O); SBAR();                                                         \
    X1(S, 10); X3; SBAR(); CV(Pn0, 3, S, 6); X1(S, 11); XT(tl0, S, 12);                         \
    SUMP(L, Pp1); SBAR();                                                                       \
    CV(Pn1, 0, S, 8); XT(tl1, S, 13); XT(tl2, S, 14); CV(Pn1, 1, S, 10); XT(tl3, S, 15);        \
  } while (0)
#define RECENTRE(NM, O, L, FE)                                                                  \
  do {                                                                                          \
    int e = __builtin_amdgcn_frexp_expf(L[0]) + (int)REF_BIAS;                                  \
    e = e > 0 ? e : 0;                                                                          \
    FE = (float)e;                                                                              \
    _Pragma("unroll") for (int r = 0; r < 16; ++r) { O[r] = __builtin_amdgcn_ldexpf(O[r], -e); NM[r] -= FE; } \
    _Pragma("unroll") for (int r = 0; r < 4; ++r) L[r] = __builtin_amdgcn_ldexpf(L[r], -e);     \
  } while (0)


constexpr int S_P1 = 0, S_P2 = 16, S_AT = 48, S_MG = 56, S_P4 = 57;

struct Ctx {
  const char *a1, *b1, *a2, *b2, *kv, *a4, *b4r, *b4i, *a5, *b5, *pf4, *pf5, *pfu;
  unsigned lds0;
  int lw, lane;
  bool isY;
};
template <int PH, int NW>
struct Loader {
  static constexpr int KA = PH <= 2 ? OP_ROW : OP_FRAG, KB = PH <= 2 ? OP_TR : PH == 4 ? OP_ROW : OP_TRP;
  static constexpr int LDA = PH == 1 ? 2048 : PH == 2 ? 4096 : PH == 4 ? 65536 : 32768, LDB = PH == 1 ? 6144 : 2048;
  static constexpr int NSUB = PH == 1 ? 3 : 2, SUBOFF = PH == 1 ? 2048 : PH == 4 ? 131072 : 128;
  static constexpr int PA = 16 / NW, PB = 8 / NW;
  unsigned va[2], vb[2];
  __device__ __forceinline__ void init(const Ctx& c) {
#pragma unroll
    for (int j = 0; j < 2; ++j) {
      va[j] = PH == 3 ? c.lane * 16 : piece_off<KA>(c.lw * PA + j, c.lane, LDA);
      vb[j] = piece_off<KB>(c.lw * PB + j, c.lane, LDB);
    }
  }
  __device__ __forceinline__ void stage(const Ctx& c, int t, int s) const {
    const unsigned d = c.lds0 + (s & 3) * SLOT;
    if constexpr (PH == 3) {
#pragma unroll
      for (int h = 0; h < 4 / NW; ++h) {
        const int part = c.lw * (4 / NW) + h;
#pragma unroll
        for (int j = 0; j < 8; ++j)
          blds<1>(mkrs(c.kv), va[0], (part & 1) * (4 << 20) + (part >> 1) * 65536 + t * 8192 + j * 1024, d + part * 8192 + j * 1024);
      }
    } else {
      const char* pa = PH == 1 ? c.a1 : PH == 2 ? c.a2 : PH == 4 ? c.a4 : c.a5;
      const char* pb = PH == 1 ? c.b1 : PH == 2 ? c.b2 : PH == 4 ? (t < 16 ? c.b4r : c.b4i) : c.b5;
      const int sa = t * (KA == OP_ROW ? 128 : 2048), sb = PH == 4 ? (t & 15) * 128 : t * 64 * LDB;
#pragma unroll
      for (int j = 0; j < PA; ++j)
        blds<(PH >= 4 ? 1 : 0)>(mkrs(pa), va[j & 1], sa + (j >> 1) * (KA == OP_ROW ? 16 * LDA : LDA), d + (c.lw * PA + j) * 1024);
#pragma unroll
      for (int u = 0; u < NSUB; ++u)
#pragma unroll
        for (int j = 0; j < PB; ++j)
          blds<0>(mkrs(pb), vb[j & 1], sb + u * SUBOFF + (j >> 1) * 16 * LDB, d + 16384 + u * 8192 + (c.lw * PB + j) * 1024);
    }
  }
};
__device__ __forceinline__ void touch(const void* p, unsigned voff, unsigned soff) {
  asm volatile("buffer_load_dword v255, %0, %1, %2 offen" ::"v"(voff), "s"(mkrs(p)), "s"(__builtin_amdgcn_readfirstlane(soff)) : "memory", "v255");
}

template <bool LD>
__device__ __forceinline__ void attention(const Ctx& c, const bf16x8 (&q)[4], const char* Qw, const char* Kh, const char* Vh, rsrc_t rO,
                                          unsigned ooff, int grp, int wq, const char* pU, gu32* uflags, gu32* myflag, unsigned tag, lptr smem) {
  const int lane = c.lane;
  const bf16x8 qa0 = q[0], qb0 = q[1], qa1 = q[2], qb1 = q[3];
  u32x4 ut;
  lptr usp = smem + wq * SLOT + 32768 + lane * 16;
#define URD(n) do { if constexpr (!LD) ut = *(const LAS u32x4*)(usp + (((t & 1) * 4 + (n)) << 10)); } while (0)
#define UST(n) do { if constexpr (!LD) st_wt(mkrs(pU, t < 2), lane * 16 + ((n) & 1) * 1024 + ((t & 1) * 2 + ((n) >> 1)) * 65536, ut); } while (0)
  Loader<3, 4> l3; Loader<4, 4> l4;
  if constexpr (LD) { l3.init(c); l4.init(c); }
  bf16x8 ones;
  {
    const __bf16 v = (((lane >> 2) ^ (lane >> 4)) & 1) ? (__bf16)0.0f : (__bf16)1.0f;
#pragma unroll
    for (int j = 0; j < 8; ++j) ones[j] = v;
  }
#define RDK(dst, P, n) dst = *(const LAS bf16x8*)((P) + (n) * 1024)
#define RDV(dst, P, n) dst = *(const LAS bf16x8*)((P) + 8192 + (n) * 1024)
  bf16x8 k0a, k0b, k1a, k1b, v0a, v0b, v1a, v1b;
  f32x16 sA, sB, nm0, nm1, o0 = {}, o1 = {};
  f32x4 l0 = {}, l1 = {};
  u32x4 pA0, pA1, pB0 = {}, pB1 = {};
  lptr kb = smem + grp * 16384 + lane * 16;
  int cur = (S_AT & 3) * SLOT;
  RDK(k0a, kb + cur, 0); RDK(k0b, kb + cur, 1); RDK(k1a, kb + cur, 2); RDK(k1b, kb + cur, 3);
  RDV(v0a, kb + cur, 0); RDV(v0b, kb + cur, 1);
  {
    const f32x16 z = {};
    f32x16 s0 = MFMA32(k0a, qa0, z); s0 = MFMA32(k0b, qb0, s0);
    f32x16 s1 = MFMA32(k1a, qa0, z); s1 = MFMA32(k1b, qb0, s1);
    nm0 = splat16(-(__builtin_ceilf(halfmax(__builtin_fmaxf(max16(s0), max16(s1)))) + REF_BIAS));
    s0 = MFMA32(k0a, qa1, z); s0 = MFMA32(k0b, qb1, s0);
    s1 = MFMA32(k1a, qa1, z); s1 = MFMA32(k1b, qb1, s1);
    nm1 = splat16(-(__builtin_ceilf(halfmax(__builtin_fmaxf(max16(s0), max16(s1)))) + REF_BIAS));
  }
  PIN(nm0); PIN(nm1);
  sA = MFMA32(k0a, qa0, nm0); sA = MFMA32(k0b, qb0, sA);
  v1a = v0a; v1b = v0a;
  int t = 0;
  float tl0 = 0.f, tl1 = 0.f, tl2 = 0.f, tl3 = 0.f;
#define NOP (void)0
#define UE(KA, KB_, VA, VB, R0, R1, X2, X3) UNIT(sA, pA0, pA1, sB, KA, KB_, qa1, qb1, nm1, o1, l1, VA, VB, pB0, pB1, R0, R1, X2, X3)
#define UO(KA, KB_, VA, VB, R0, R1, X2, X3) UNIT(sB, pB0, pB1, sA, KA, KB_, qa0, qb0, nm0, o0, l0, VA, VB, pA0, pA1, R0, R1, X2, X3)
#pragma unroll 1
  while (t < 8) {
    bool trig;
#pragma unroll 1
    do {
      lptr cp = kb + cur;
      cur = cur == 3 * SLOT ? 0 : cur + SLOT;
      lptr np = kb + cur;
      SBAR();
      UE(k0a, k0b, v1a, v1b, RDK(k1a, cp, 2), RDK(k1b, cp, 3), URD(0), UST(0));
      UO(k1a, k1b, v0a, v0b, RDV(v1a, cp, 2), RDV(v1b, cp, 3), URD(1), UST(1));
      UE(k1a, k1b, v0a, v0b, RDK(k0a, cp, 4), RDK(k0b, cp, 5), URD(2), UST(2));
      UO(k0a, k0b, v1a, v1b, RDV(v0a, cp, 4), RDV(v0b, cp, 5), URD(3), UST(3));
      UE(k0a, k0b, v1a, v1b, RDK(k1a, cp, 6), RDK(k1b, cp, 7), NOP, NOP);
      UO(k1a, k1b, v0a, v0b, RDV(v1a, cp, 6), RDV(v1b, cp, 7), NOP, NOP);
      if constexpr (LD) {
        drain();
        if (t == UW && wq == 2 && c.isY) await(uflags, lane & 15, tag, lane, smem);
        bar_l();
        if (t < 4) l3.stage(c, t + 4, S_AT + t + 4);
        else if (t >= UW) {
          if (c.isY) {
#pragma unroll 1
            for (int k = t == UW ? 0 : t - 5; k <= t - 5; ++k) l4.stage(c, k, S_P4 + k);
          }
        }
        else if (t == UW - 1 && wq == 2) mail(uflags, lane & 15, c.lds0);
        if (t == 2) set_flag(myflag, tag);
        if (t == TP) { touch(c.pf4, lane * 128, wq * 8192); touch(c.pf5, lane * 128, wq * 8192); }
        if (t == TU && !c.isY) {
#pragma unroll
          for (int k = 0; k < 2; ++k) touch(c.pfu, (lane >> 1) * 2048 + (lane & 1) * 128, (wq * 2 + k) * 65536);
        }
      } else {
        bar_vl<4>();
      }
      SBAR();
      UE(k1a, k1b, v0a, v0b, RDK(k0a, np, 0), RDK(k0b, np, 1), NOP, NOP);
      UO(k0a, k0b, v1a, v1b, RDV(v0a, np, 0), RDV(v0b, np, 1); trig = __any(l0[0] + l1[0] > L_LIMIT), NOP, NOP);
      ++t;
    } while (t < 8 && !trig);
    if (t >= 8) break;
    asm volatile("s_nop 7" : "+v"(l0), "+v"(l1));
    pB1[2] = pk(tl0, tl1); pB1[3] = pk(tl2, tl3);
    o1 = MFMA32(v1a, BC(pB0), o1); o1 = MFMA32(v1b, BC(pB1), o1);
    l1 = MFMA16(ones, BC(pB0), l1); l1 = MFMA16(ones, BC(pB1), l1);
    pB0 = u32x4{}; pB1 = u32x4{}; tl0 = 0.f; tl1 = 0.f; tl2 = 0.f; tl3 = 0.f;
    float f0, f1;
    RECENTRE(nm0, o0, l0, f0);
    RECENTRE(nm1, o1, l1, f1);
#pragma unroll
    for (int r = 0; r < 16; ++r) sA[r] -= f0;
    asm volatile("s_nop 1" : "+v"(l0), "+v"(l1), "+v"(pB0), "+v"(pB1));
  }
  asm volatile("s_nop 7" : "+v"(l0), "+v"(l1));
  pB1[2] = pk(tl0, tl1); pB1[3] = pk(tl2, tl3);
  o1 = MFMA32(v1a, BC(pB0), o1); o1 = MFMA32(v1b, BC(pB1), o1);
  l1 = MFMA16(ones, BC(pB0), l1); l1 = MFMA16(ones, BC(pB1), l1);
  lptr mg = smem + (S_MG & 3) * SLOT + lane * 16;
  {
    const f32x16& og = LD ? o0 : o1;
    lptr d = mg + ((1 - grp) * 4 + wq) * 5120;
#pragma unroll
    for (int q = 0; q < 4; ++q) *(LAS f32x4*)(d + q * 1024) = f32x4{og[4 * q], og[4 * q + 1], og[4 * q + 2], og[4 * q + 3]};
    *(LAS f32x4*)(d + 4096) = f32x4{LD ? l0[0] : l1[0], LD ? nm0[0] : nm1[0], 0.f, 0.f};
  }
  bar_l();
  f32x16 o = LD ? o1 : o0;
  float l = LD ? l1[0] : l0[0];
  {
    lptr d = mg + (grp * 4 + wq) * 5120;
    const f32x4 e = *(const LAS f32x4*)(d + 4096);
    const float nmk = LD ? nm1[0] : nm0[0], nmin = __builtin_fminf(nmk, e[1]);
    const int ek = (int)__builtin_fmaxf(nmin - nmk, -400.f), eo = (int)__builtin_fmaxf(nmin - e[1], -400.f);
    l = __builtin_amdgcn_ldexpf(l, ek) + __builtin_amdgcn_ldexpf(e[0], eo);
    const float inv = 1.0f / l, fk = __builtin_amdgcn_ldexpf(inv, ek), fo = __builtin_amdgcn_ldexpf(inv, eo);
#pragma unroll
    for (int q = 0; q < 4; ++q) {
      const f32x4 x = *(const LAS f32x4*)(d + q * 1024);
#pragma unroll
      for (int r = 0; r < 4; ++r) o[4 * q + r] = __builtin_fmaf(o[4 * q + r], fk, x[r] * fo);
    }
  }
  float chk = l;
#pragma unroll
  for (int r = 0; r < 16; ++r) chk += __builtin_fabsf(o[r]);
  u32x4 x, y;
  if (__builtin_expect(__any(!(chk < 3.0e38f) || !(l > 0.f)), 0)) attn_safe(Kh, Vh, Qw + grp * 2048, lane, x, y);
  else pack16(o, 1.0f, x, y);
  const unsigned vo = ooff + grp * 65536 + ((lane >> 4) & 1) * 32768 + (lane >> 5) * 256 + (lane & 15) * 16;
  st_wt(rO, vo, x); st_wt(rO, vo + 512, y);
}

__device__ __forceinline__ void store_out(const f32x4 (&acc)[4][4], u16* out, int row0, int col0, int lane) {
#pragma unroll
  for (int mi = 0; mi < 4; ++mi) {
    const u32x4 c0 = natural(pack2(acc[mi][0], acc[mi][1], 1.0f)), c1 = natural(pack2(acc[mi][2], acc[mi][3], 1.0f));
    u32x4 d0, d1;
#pragma unroll
    for (int r = 0; r < 4; ++r) {
      d0[r] = __builtin_amdgcn_update_dpp(c0[r], c1[r], 0x128, 0xf, 0xc, false);
      d1[r] = __builtin_amdgcn_update_dpp(c1[r], c0[r], 0x128, 0xf, 0x3, false);
    }
    const unsigned vo = ((row0 + (lane & 7)) * 2048 + col0 + ((lane >> 3) & 1) * 32 + (lane >> 4) * 8) * 2;
    __builtin_amdgcn_raw_buffer_store_b128(d0, mkrs(out), vo, mi * 65536, 16);
    __builtin_amdgcn_raw_buffer_store_b128(d1, mkrs(out), vo, mi * 65536 + 32768, 16);
  }
}

__global__ __launch_bounds__(512, 2) void fused(const u16* __restrict__ xA, const u16* __restrict__ Wsr, const u16* __restrict__ Wsi,
                                                const u16* __restrict__ Wdr, const u16* __restrict__ Wdi, const u16* __restrict__ xB,
                                                const u16* __restrict__ wqkv, const u16* __restrict__ wo, u16* __restrict__ out,
                                                char* ws) {
  extern __shared__ __attribute__((aligned(16))) char smem_[];
  lptr smem = (lptr)smem_;
  const int lane = threadIdx.x & 63, w = __builtin_amdgcn_readfirstlane(threadIdx.x >> 6), wm = (w >> 1) & 1, wn = w & 1;
  const int xcd = blockIdx.x & 7, i = blockIdx.x >> 3;
  gu32* flags = (gu32*)ws + 64;
  char* QKV = ws + (1 << 20);
  char* Of = QKV + (12 << 20);
  char* Uf = Of + (4 << 20);
  const int j2 = xcd * 2 + (i >> 4), tb1 = i & 15;
  const int tbA = xcd * 2 + (i >> 4), im = (i >> 3) & 1, nb2 = i & 7;
  const int head = xcd * 4 + (i >> 3), qb = i & 7;
  const bool isY = i < 16;
  const int tb5 = (xcd >> 1) * 4 + (i & 3), nb5 = (xcd & 1) * 4 + ((i >> 2) & 3);
  const char* Kh = QKV + (4 << 20) + head * 131072;
  const char* Vh = QKV + (8 << 20) + head * 131072;
  const int qt = qb * 8 + (w & 3) * 2;
  const char* Qw = QKV + (head * 64 + qt) * 2048;
  const unsigned ooff = qt * 65536 + head * 1024;
  Ctx c;
  c.lane = lane; c.lw = w; c.lds0 = (unsigned)(unsigned long)smem;
  c.a1 = (const char*)(xB + tb1 * 128 * D_); c.b1 = (const char*)(wqkv + j2 * 64);
  {
    Loader<1, 8> l0; l0.init(c);
    l0.stage(c, 0, 0);
  }
  const void* p1 = w == 7 ? (const void*)c.b1 : (const void*)(xB + blockIdx.x * 8 * D_);
  const unsigned vo1 = w == 7 ? (lane < 48 ? (tb1 * 16 + (lane & 15)) * 6144 + (lane >> 4) * 2048 : 0u) : ((lane >> 2) & 7) * 2048 + (lane & 3) * 128;
  const unsigned qs1 = w == 7 ? 256 * 6144 : 512;
  if (w >= 6) {
#pragma unroll
    for (int k = 0; k < NQE; ++k) touch(p1, vo1, k * qs1);
  }
  SBAR();
  c.lw = w & 3;
  if (__builtin_expect(w >= 4, 1)) {
    c.a2 = (const char*)((im ? Wsi : Wsr) + tbA * 128 * S_); c.b2 = (const char*)(xA + nb2 * 128);
    c.kv = Kh;
    c.a4 = Uf + tb5 * 8 * 65536; c.b4r = (const char*)(Wdr + nb5 * 128 * D_); c.b4i = (const char*)(Wdi + nb5 * 128 * D_);
    c.pf4 = (const char*)Wdr + ((xcd & 1) * 32 + i) * 32768; c.pf5 = (const char*)Wdi + ((xcd & 1) * 32 + i) * 32768;
    c.a5 = Of + tb5 * 8 * 32768; c.b5 = (const char*)(wo + nb5 * 128);
    c.isY = isY;
    c.pfu = c.b5 + (i & 3) * 256 * 2048;
    gu32 *fq = flags + (head >> 1) * 16, *fo = flags + 512 + (tb5 >> 1) * 32;
    unsigned tag = 0;
    bf16x8 q[4];
    auto load_q = [&]() {
#pragma unroll
      for (int k = 0; k < 4; ++k) blds<1>(mkrs(Qw), lane * 16, k * 1024, c.lds0 + c.lw * SLOT + 32768 + k * 1024);
    };
    auto read_q = [&]() {
#pragma unroll
      for (int k = 0; k < 4; ++k) q[k] = *(const LAS bf16x8*)(smem + c.lw * SLOT + 32768 + k * 1024 + lane * 16);
    };
    if (__builtin_expect(w < 6, 1)) {
      {
        Loader<1, 2> l1; l1.init(c);
        l1.stage(c, 1, 1);
#pragma unroll 1
        for (int s = -1; s <= CUP; ++s) { bar_v<20>(); l1.stage(c, s + 3, s + 3); }
        l1.stage(c, CUP + 4, CUP + 4);
#pragma unroll 1
        for (int s = CUP + 1; s < 12; ++s) { bar_v<40>(); l1.stage(c, s + 4, s + 4); }
      }
      {
        Loader<2, 2> l2; l2.init(c);
#pragma unroll 1
        for (int s = 12; s < 44; ++s) { bar_v<32>(); l2.stage(c, s - 12, s + 4); }
      }
      Loader<3, 2> l3; l3.init(c);
      bar_v<32>();
      load_q();
      l3.stage(c, 0, 48);
#pragma unroll 1
      for (int s = 45; s < 47; ++s) { bar_v<32>(); l3.stage(c, s - 44, s + 4); }
      asm volatile("s_waitcnt vmcnt(32)" ::: "memory");
      read_q();
      bar_l();
      l3.stage(c, 3, 51);
    } else {
      const bool u = w == 7;
      const int b = blockIdx.x;
      const unsigned vo2 = (lane >> 2) * 4096 + (lane & 3) * 128;
      const char* pKVo = QKV + (4 << 20) + ((j2 * 2) * 64 + tb1 * 4 + (w & 1) * 2) * 2048;
      bar_v<NQE>();
#pragma unroll 1
      for (int s = 0; s < 44; ++s) {
        if (s == FL) drain();
        bar_l();
#pragma unroll
        for (int k = NQE; k < 4; ++k)
          if (s == QS) {
            if (nb2 < QO || !isY) touch(p1, vo1, k * qs1);
            if (nb2 >= QO && !isY) touch((const char*)p1 - (u ? 128 : 128 * 8 * 2048), vo1, k * qs1);
          }
        if (s == CS && !u) touch(ws, 0, 0);
        if (s == 12) {
          asm volatile("s_load_dword %0, %1, 0x0\n\ts_waitcnt lgkmcnt(0)" : "=s"(tag) : "s"(ws) : "memory");
          tag = tag * 1664525u + 1013904223u;
        }
        if (s == PWS + PWI * nb2 && !isY) {
#pragma unroll
          for (int k = 0; k < 2; ++k) touch(xA + (nb2 * 256 + (xcd * 2 + im) * 16 + (w & 1) * 8) * D_, lane * 128, k * 8192);
#pragma unroll
          for (int k = 0; k < 8; ++k) touch(c.a2 + nb2 * 512 - (w & 1) * 128 * 4096, vo2, k * 65536);
        }
        if (s >= 16 && s < 16 + KVN) {
#pragma unroll
          for (int m = 0; m < 16 / KVN; ++m) {
            const int ix = (s - 16) * (16 / KVN) + m, n = ix >> 1, h = ix & 1;
            const u32x4 v = *(const LAS u32x4*)(smem + ((w & 1) * 2 + h) * SLOT + 32768 + n * 1024 + lane * 16);
            __builtin_amdgcn_raw_buffer_store_b128(v, mkrs(pKVo), lane * 16, h * 131072 + (n >> 2) * (4 << 20) + (n & 3) * 1024, 16);
          }
        }
        if (s == FL) set_flag(flags + j2 * 16 + tb1, tag);
        if (s == 40 && !u) mail(fq, lane & 15, c.lds0);
      }
      drain();
      if (!u) await(fq, lane & 15, tag, lane, smem);
      bar_l();
      load_q();
      bar_l(); bar_l();
      drain();
      read_q();
      bar_l();
      touch(u ? Wdi : Wdr, lane * 128, b * 8192);
      touch(wo, (lane & 31) * 128, b * 8192 + (u ? 4096 : 0));
    }
    attention<true>(c, q, Qw, Kh, Vh, mkrs(Of), ooff, 1, w & 3, nullptr, flags + 1024 + tb5 * 16, flags + 1024 + tbA * 16 + (i & 15), tag, smem);
    if (!isY) {
      bar_vl<0>();
      if (w < 6) {
        Loader<5, 2> l5; l5.init(c);
        bar_l();
        l5.stage(c, 0, S_P4); l5.stage(c, 2, S_P4 + 1);
        bar_v<16>();
        l5.stage(c, 4, S_P4 + 2);
#pragma unroll 1
        for (int p = 3; p < 16; ++p) { l5.stage(c, p < 8 ? 2 * p : 2 * p - 15, S_P4 + p); bar_v<32>(); }
        bar_v<16>(); bar_v<0>(); bar_v<0>();
      } else if (w == 6) {
        set_flag(flags + 512 + qb * 32 + head, tag);
        mail(fo, lane & 29, c.lds0); drain(); await(fo, lane & 29, tag, lane, smem);
        bar_l(); bar_l(); bar_l(); bar_l();
        mail(fo, lane & 31, c.lds0);
        bar_l(); bar_l();
        drain(); await(fo, lane & 31, tag, lane, smem);
        bar_l();
      }
      return;
    }
    bar_vl<2>();
    if (w < 6) {
      Loader<4, 2> l4; l4.init(c);
      l4.stage(c, 3, S_P4 + 3);
#pragma unroll 1
      for (int t = 4; t < 6; ++t) { bar_v<34>(); l4.stage(c, t, S_P4 + t); }
#pragma unroll 1
      for (int t = 6; t < 32; ++t) { bar_v<32>(); l4.stage(c, t, S_P4 + t); }
      bar_v<32>(); bar_v<16>(); bar_v<0>(); bar_v<0>();
    } else {
#pragma unroll 1
      for (int s = S_P4; s < S_P4 + 17; ++s) {
        if (s == S_P4 + OFL) drain();
        bar_l();
        if (s == S_P4 + OFL) set_flag(flags + 512 + qb * 32 + head, tag);
        if (blockIdx.x == 0 && w == 6) {
          if (s == S_P4 + 8) blds<1>(mkrs((const void*)flags), lane * 16, 0, c.lds0 + MAIL + 1024);
          if (s == S_P4 + 16) {
            drain();
            u32x4 f = *(volatile LAS u32x4*)(smem + MAIL + 1024 + lane * 16);
#pragma unroll 1
            for (unsigned spins = 0; spins < 400000u && !__all(f[0] == tag && f[1] == tag && f[2] == tag && f[3] == tag); ++spins) {
              __builtin_amdgcn_s_sleep(2);
              f = __builtin_amdgcn_raw_buffer_load_b128(mkrs((const void*)flags), lane * 16, 0, 16);
            }
            set_flag((gu32*)ws, tag);
          }
        }
      }
    }
    return;
  }
  const unsigned lds0 = c.lds0;
  lptr park = smem + w * SLOT + 32768;
  bar_v<0>();
  {
    FragRd<OP_ROW, 1> ra; ra.init(wm * 4, lane, lds0 + (S_P1 & 3) * SLOT);
    FragRd<OP_TR, 2> rb; rb.init(wn * 2, lane, lds0 + (S_P1 & 3) * SLOT + 16384);
    f32x4 acc[4][6] = {};
    consume<4, 6, 4, S_P1, 16, 0>(acc, ra, rb);
    const rsrc_t rQ = mkrs(QKV + ((j2 * 2 + wn) * 64 + tb1 * 4 + wm * 2) * 2048);
    const int cc = lane & 15, g = lane >> 4;
#pragma unroll
    for (int mi = 0; mi < 4; ++mi) {
      const unsigned blk = (mi >> 1) * 2048 + g * 512;
      st_wt(rQ, blk + ((mi & 1) * 16 + cc) * 16, pack2(acc[mi][0], acc[mi][1], 0.17677669529663687f * 1.4426950408889634f));
      *(LAS u32x4*)(park + blk + ((cc >> 3) * 16 + (mi & 1) * 8 + (cc & 7)) * 16) = pack2(acc[mi][2], acc[mi][3], 1.0f);
      *(LAS u32x4*)(park + 4096 + blk + ((mi & 1) * 16 + cc) * 16) = pack2(acc[mi & 2][4 + (mi & 1)], acc[(mi & 2) + 1][4 + (mi & 1)], 1.0f);
    }
  }
  bf16x8 q[4];
  {
    FragRd<OP_ROW, 1> ra; ra.init(wm * 4, lane, lds0 + (S_P2 & 3) * SLOT);
    FragRd<OP_TR, 4> rb; rb.init(0, lane, lds0 + (S_P2 & 3) * SLOT + 16384 + wn * 8192);
    f32x4 acc[4][4] = {};
    consume<4, 4, 4, S_P2, 32, 11>(acc, ra, rb);
#pragma unroll
    for (int k = 0; k < 4; ++k) q[k] = *(const LAS bf16x8*)(park + k * 1024 + lane * 16);
    const float sc = im ? -1.0f : 1.0f;
#pragma unroll
    for (int mi = 0; mi < 4; ++mi)
#pragma unroll
      for (int n2 = 0; n2 < 2; ++n2)
        *(LAS u32x4*)(park + (mi * 2 + n2) * 1024 + lane * 16) = natural(pack2(acc[mi][2 * n2], acc[mi][2 * n2 + 1], sc));
  }
  attention<false>(c, q, Qw, Kh, Vh, mkrs(Of), ooff, 0, w, Uf + (tbA * 8 + wm * 4) * 65536 + (im * 32 + nb2 * 4 + wn * 2) * 1024,
                   nullptr, nullptr, 0u, smem);
  FragRd<OP_FRAG, 1> ra; ra.init(wm * 4, lane, lds0 + (S_P4 & 3) * SLOT);
  f32x4 acc[4][4] = {};
  if (isY) {
    FragRd<OP_ROW, 1> rb; rb.init(wn * 4, lane, lds0 + (S_P4 & 3) * SLOT + 16384);
    bar_l();
    consume<4, 4, 4, S_P4, 32, OFL>(acc, ra, rb);
  } else {
    FragRd<OP_TRP, 4> rb; rb.init(0, lane, lds0 + (S_P4 & 3) * SLOT + 16384 + wn * 8192);
    bar_vl<0>(); bar_l(); bar_l();
    consume<4, 4, 4, S_P4, 16, 0>(acc, ra, rb);
  }
  store_out(acc, out, tb5 * 128 + wm * 64, (isY ? 0 : 1024) + nb5 * 128 + wn * 64, lane);
}

extern "C" void kernel_launch(void* const* d_in, const int* in_sizes, int n_in, void* d_out, int out_size, void* d_ws,
                              size_t ws_size, hipStream_t stream) {
  (void)hipFuncSetAttribute((const void*)fused, hipFuncAttributeMaxDynamicSharedMemorySize, LDS_BYTES);
  fused<<<256, 512, LDS_BYTES, stream>>>((const u16*)d_in[0], (const u16*)d_in[1], (const u16*)d_in[2], (const u16*)d_in[3],
                                         (const u16*)d_in[4], (const u16*)d_in[5], (const u16*)d_in[6], (const u16*)d_in[7],
                                         (u16*)d_out, (char*)d_ws);
}
```
